```python
import torch

def _softmax(x, axis=-1):
    return torch.softmax(x, dim=axis)

def _gelu(x):
    return torch.nn.functional.gelu(x, approximate='tanh')

def _silu(x):
    return torch.nn.functional.silu(x)

def _sigmoid(x):
    return torch.sigmoid(x)

def _layernorm(x, axis=-1, eps=1e-05):
    m = x.mean(dim=axis, keepdim=True)
    v = x.var(dim=axis, keepdim=True, unbiased=False)
    return (x - m) / torch.sqrt(v + eps)

def _scatter_add(tgt, idx, src, axis=0):
    return tgt.clone().index_add_(axis, idx, src)

def _segment_sum(src, seg, n):
    out = torch.zeros((n,) + src.shape[1:], dtype=src.dtype)
    return out.index_add_(0, seg, src)

def _pad(x, pw):
    flat = tuple((v for pair in reversed(pw) for v in pair))
    return torch.nn.functional.pad(x, flat)
kernel_name = 'parallel_sum__memory_aug_recurrent_mem_tr__mixer_channel_mlp__prompt'

def setup_inputs():

    _w = lambda a: (lambda t: t.to(torch.bfloat16) if t.is_floating_point() else t)(torch.as_tensor(a))

    def _A_setup():
        B, S, Dm = (1, 2048, 512)
        H, Dh = (32, 128)
        M, seg = (8, 32)
        seg = min(seg, S)
        S = S // seg * seg
        return {'x': _w((lambda *s: torch.randn(*s, dtype=torch.bfloat16) * 0.05)(B, S, Dm)), 'mem0': _w((lambda *s: torch.randn(*s, dtype=torch.bfloat16) * 0.2)(B, M, Dm)), 'Wq': _w((lambda *s: torch.randn(*s, dtype=torch.bfloat16) * 1.6)(Dm, H, Dh) * Dm ** (-0.5)), 'Wk': _w((lambda *s: torch.randn(*s, dtype=torch.bfloat16) * 1.6)(Dm, H, Dh) * Dm ** (-0.5)), 'Wv': _w((lambda *s: torch.randn(*s, dtype=torch.bfloat16) * 0.2)(Dm, H, Dh) * Dm ** (-0.5)), 'Wo': _w((lambda *s: torch.randn(*s, dtype=torch.bfloat16) * 0.2)(H, Dh, Dm) * (H * Dh) ** (-0.5)), 'seg_len': seg}
    _A_in = _A_setup()

    def _B_setup():
        B, S, D, F = (1, 2048, 512, 1792)
        return {'x': _w((lambda *s: torch.randn(*s, dtype=torch.bfloat16) * 0.05)(B, S, D)), 'w1': _w((lambda *s: torch.randn(*s, dtype=torch.bfloat16) * 0.8)(D, F) * D ** (-0.5)), 'b1': _w((lambda *s: torch.randn(*s, dtype=torch.bfloat16) * 0.4)(F)), 'w2': _w((lambda *s: torch.randn(*s, dtype=torch.bfloat16) * 0.05)(F, D) * F ** (-0.5)), 'b2': _w((lambda *s: torch.randn(*s, dtype=torch.bfloat16) * 0.05)(D))}
    _B_in = _B_setup()
    return {'A_x': _A_in['x'], 'A_mem0': _A_in['mem0'], 'A_Wq': _A_in['Wq'], 'A_Wk': _A_in['Wk'], 'A_Wv': _A_in['Wv'], 'A_Wo': _A_in['Wo'], 'A_seg_len': _A_in['seg_len'], 'B_x': _B_in['x'], 'B_w1': _B_in['w1'], 'B_b1': _B_in['b1'], 'B_w2': _B_in['w2'], 'B_b2': _B_in['b2']}

def reference(A_x, A_mem0, A_Wq, A_Wk, A_Wv, A_Wo, A_seg_len, B_x, B_w1, B_b1, B_w2, B_b2):

    def _A_ref(x, mem0, Wq, Wk, Wv, Wo, seg_len):
        B, S, Dm = x.shape
        H, Dh = (Wq.shape[1], Wq.shape[2])
        M = mem0.shape[1]
        scale = Dh ** (-0.5)
        n_seg = S // seg_len
        L = M + seg_len
        idx = torch.arange(L)
        mask = (idx[:, None] < M) | (idx[:, None] >= idx[None, :])
        mem = mem0
        outs = []
        for s in range(n_seg):
            xs = x[:, s * seg_len:(s + 1) * seg_len, :].clone()
            h = torch.concatenate([mem, xs], axis=1)
            q = torch.einsum('btd,dhe->bhte', h, Wq)
            k = torch.einsum('btd,dhe->bhte', h, Wk)
            v = torch.einsum('btd,dhe->bhte', h, Wv)
            scores = torch.einsum('bhqe,bhke->bhqk', q, k) * scale
            scores = torch.where(mask[None, None], scores, -1000000000.0)
            p = _softmax(scores, axis=-1)
            o = torch.einsum('bhqk,bhke->bhqe', p, v)
            o = torch.einsum('bhte,hed->btd', o, Wo)
            mem = o[:, :M, :].clone()
            outs.append(o[:, M:, :])
        return torch.concatenate(outs, axis=1)

    def _B_ref(x, w1, b1, w2, b2):
        y = _layernorm(x, axis=-1)
        h = _gelu(torch.einsum('bsd,df->bsf', y, w1) + b1)
        return torch.einsum('bsf,fd->bsd', h, w2) + b2
    x = A_x
    xn = _layernorm(x, axis=-1)
    return x + _A_ref(xn, A_mem0, A_Wq, A_Wk, A_Wv, A_Wo, A_seg_len) + _B_ref(xn, B_w1, B_b1, B_w2, B_b2)
```

```cpp
#include <hip/hip_runtime.h>
#include <hip/hip_bf16.h>
#include <stdlib.h>

typedef unsigned v4u __attribute__((ext_vector_type(4)));
typedef unsigned v2u __attribute__((ext_vector_type(2)));
typedef float f32x4 __attribute__((ext_vector_type(4)));
typedef __bf16 bf16x2 __attribute__((ext_vector_type(2)));
typedef __bf16 bf16x4 __attribute__((ext_vector_type(4)));
typedef __bf16 bf16x8 __attribute__((ext_vector_type(8)));
#define LAS __attribute__((address_space(3)))
#define GAS __attribute__((address_space(1)))
typedef __amdgpu_buffer_rsrc_t rsrc_t;

constexpr int S_ = 2048, D_ = 512, H_ = 32, NSEG = 64, F_ = 1792;
constexpr int LDS_BYTES = 160 * 1024;
constexpr unsigned SPIN_CAP = 400000u;

constexpr unsigned FLAG_BYTES = 65536;
constexpr unsigned FL_TAG = 0, FL_LN = 1, FL_K = 129, FL_V = 161, FL_Q = 193, FL_M1 = 225, FL_M2 = 239, FL_C = 255, FL_X = 319, FL_BAR = 383,
                   FL_DEC = 447, FL_TMO = 450;
constexpr unsigned TAG_STEP = 0x9E3779B9u;
#define KV_PERM 0
#define TOUCH 1
#define PROBE_N 5
#define START_DELAY 6
#define PARITY_FLIP 0u
constexpr unsigned MB = 1u << 20;
constexpr unsigned O_MEM = 0, O_PART = 4 * MB, O_XN = 6 * MB, O_Y = 8 * MB, O_KX = 10 * MB, O_VX = 26 * MB, O_QX = 42 * MB,
                   O_KM = 58 * MB, O_VM = 62 * MB, O_H = 66 * MB, O_XP = 74 * MB, O_END = 146 * MB;

#define STAMPM(m, slot, i) do {} while (0)
#define STAMP_DECL do {} while (0)
#define STAMPW(slot, i) do {} while (0)

struct Params {
  const __bf16* x; const __bf16* mem0; const __bf16* Wq; const __bf16* Wk; const __bf16* Wv; const __bf16* Wo;
  const __bf16* w1; const __bf16* b1; const __bf16* w2; const __bf16* b2;
  __bf16* out; char* flags; char* ws;
};

__device__ __forceinline__ v4u ld16(rsrc_t r, unsigned off) { return __builtin_amdgcn_raw_buffer_load_b128(r, off, 0, 16); }
__device__ __forceinline__ unsigned ld4(rsrc_t r, unsigned off) { return __builtin_amdgcn_raw_buffer_load_b32(r, off, 0, 16); }
__device__ __forceinline__ void st16(rsrc_t r, unsigned off, v4u v) { __builtin_amdgcn_raw_buffer_store_b128(v, r, off, 0, 16); }
__device__ __forceinline__ void st4(rsrc_t r, unsigned off, unsigned v) { __builtin_amdgcn_raw_buffer_store_b32(v, r, off, 0, 16); }
__device__ __forceinline__ void ld16_async(v4u& v, rsrc_t r, unsigned off) { asm volatile("buffer_load_dwordx4 %0, %1, %2, 0 offen sc1" : "=v"(v) : "v"(off), "s"(r) : "memory"); }
__device__ __forceinline__ void waitvm0() { asm volatile("s_waitcnt vmcnt(0)" ::: "memory"); }
__device__ __forceinline__ void bar() { asm volatile("s_waitcnt lgkmcnt(0)" ::: "memory"); __builtin_amdgcn_s_barrier(); asm volatile("" ::: "memory"); }
__device__ __forceinline__ unsigned pk2(float a, float b) { bf16x2 v; v[0] = (__bf16)a; v[1] = (__bf16)b; return __builtin_bit_cast(unsigned, v); }
__device__ __forceinline__ float bfr(float a) { return (float)(__bf16)a; }
__device__ __forceinline__ float lo16(unsigned u) { return __uint_as_float(u << 16); }
__device__ __forceinline__ float hi16(unsigned u) { return __uint_as_float(u & 0xffff0000u); }
__device__ __forceinline__ f32x4 mfma(v4u a, v4u b, f32x4 c) { return __builtin_amdgcn_mfma_f32_16x16x32_bf16(__builtin_bit_cast(bf16x8, a), __builtin_bit_cast(bf16x8, b), c, 0, 0, 0); }
__device__ __forceinline__ v4u lds16(const LAS char* p) { return *(const LAS v4u*)p; }
__device__ __forceinline__ v2u ldstr(const LAS char* p) { return __builtin_bit_cast(v2u, __builtin_amdgcn_ds_read_tr16_b64_v4bf16((LAS bf16x4*)p)); }
__device__ __forceinline__ float xmax16(float a) { auto r = __builtin_amdgcn_permlane16_swap(__float_as_uint(a), __float_as_uint(a), false, false); return fmaxf(__uint_as_float(r[0]), __uint_as_float(r[1])); }
__device__ __forceinline__ float xmax32(float a) { auto r = __builtin_amdgcn_permlane32_swap(__float_as_uint(a), __float_as_uint(a), false, false); return fmaxf(__uint_as_float(r[0]), __uint_as_float(r[1])); }
__device__ __forceinline__ float xsum16(float a) { auto r = __builtin_amdgcn_permlane16_swap(__float_as_uint(a), __float_as_uint(a), false, false); return __uint_as_float(r[0]) + __uint_as_float(r[1]); }
__device__ __forceinline__ float xsum32(float a) { auto r = __builtin_amdgcn_permlane32_swap(__float_as_uint(a), __float_as_uint(a), false, false); return __uint_as_float(r[0]) + __uint_as_float(r[1]); }

__device__ __forceinline__ unsigned load_epoch(rsrc_t fr) { return __builtin_amdgcn_readfirstlane(ld4(fr, FL_TAG * 128u)); }
__device__ __forceinline__ unsigned tag_of(unsigned epoch) { return epoch * 0x9E3779B1u + 0x7F4A7C15u; }
__device__ __forceinline__ void set_flag(rsrc_t fr, unsigned line, unsigned count, unsigned tag) {
  __builtin_amdgcn_raw_buffer_store_b64((v2u){count, tag}, fr, line * 128u, 0, 16);
}
__device__ __forceinline__ v2u get_flag(rsrc_t fr, unsigned line) { return __builtin_amdgcn_raw_buffer_load_b64(fr, line * 128u, 0, 16); }
__device__ __forceinline__ void wait_count(rsrc_t fr, unsigned tag, int first, int n, unsigned want, unsigned& known, int lane, bool& dead) {
  if (known >= want || dead) return;
  const unsigned line = (unsigned)(first + (lane < n ? lane : 0));
  for (unsigned spins = 0;; ++spins) {
    asm volatile("" ::: "memory");
    const v2u f = get_flag(fr, line);
    unsigned v = f[1] == tag ? f[0] : 0u;
    if (__all(v >= want)) {
#pragma unroll
      for (int m = 32; m >= 1; m >>= 1) v = min(v, (unsigned)__shfl_xor((int)v, m));
      known = __builtin_amdgcn_readfirstlane(v);
      return;
    }
    if (spins > SPIN_CAP) { st4(fr, FL_TMO * 128u, 1u); dead = true; return; }
    __builtin_amdgcn_s_sleep(1);
  }
}
__device__ __forceinline__ void wait_ln(rsrc_t fr, unsigned tag, int s, unsigned& known, int lane, bool& dead) {
  if (known > (unsigned)s || dead) return;
  for (unsigned spins = 0;; ++spins) {
    asm volatile("" ::: "memory");
    const v2u f0 = get_flag(fr, FL_LN + 2 * lane), f1 = get_flag(fr, FL_LN + 2 * lane + 1);
    const unsigned long long miss = ~__ballot(f0[1] == tag && f1[1] == tag);
    const unsigned lead = miss ? (unsigned)__builtin_ctzll(miss) : 64u;
    if (lead > (unsigned)s) { known = lead; return; }
    if (spins > SPIN_CAP) { st4(fr, FL_TMO * 128u, 1u); dead = true; return; }
    __builtin_amdgcn_s_sleep(1);
  }
}

__device__ __forceinline__ void stage_rows(const __bf16* src, size_t ld, int rows, int rowbytes, LAS char* dst, int wave, int lane) {
  const int rpi = 1024 / rowbytes;
  const int lpr = rowbytes / 16;
  const int n = rows / rpi;
#pragma unroll 8
  for (int i = wave; i < n; i += 4) {
    const __bf16* g = src + (size_t)(i * rpi + lane / lpr) * ld + (lane % lpr) * 8;
    __builtin_amdgcn_global_load_lds((const GAS unsigned*)g, (LAS unsigned*)(dst + i * 1024), 16, 0, 0);
  }
  waitvm0();
  bar();
}

__device__ __forceinline__ float row16_sum(float v) {
  v += __uint_as_float(__builtin_amdgcn_update_dpp(0u, __float_as_uint(v), 0x128, 0xf, 0xf, false));
  v += __uint_as_float(__builtin_amdgcn_update_dpp(0u, __float_as_uint(v), 0x124, 0xf, 0xf, false));
  v += __uint_as_float(__builtin_amdgcn_update_dpp(0u, __float_as_uint(v), 0x122, 0xf, 0xf, false));
  v += __uint_as_float(__builtin_amdgcn_update_dpp(0u, __float_as_uint(v), 0x121, 0xf, 0xf, false));
  return v;
}
__device__ __forceinline__ void ln32(float (&v)[32]) {
  float s = 0.f;
#pragma unroll
  for (int i = 0; i < 32; ++i) s += v[i];
  const float mean = row16_sum(s) * (1.f / 512.f);
  float q = 0.f;
#pragma unroll
  for (int i = 0; i < 32; ++i) { const float d = v[i] - mean; q += d * d; }
  const float var = bfr(row16_sum(q) * (1.f / 512.f));
  const float mb = bfr(mean);
  const float rs = 1.f / bfr(sqrtf(bfr(var + 1e-5f)));
#pragma unroll
  for (int i = 0; i < 32; ++i) v[i] = bfr(bfr(v[i] - mb) * rs);
}
__device__ __forceinline__ void role_ln(const Params& P, rsrc_t wr, rsrc_t fr, unsigned tag, int half_seg, int wave, int lane) {
  const int row = half_seg * 16 + wave * 4 + (lane >> 4), li = lane & 15;
  float v[32];
#pragma unroll
  for (int i = 0; i < 4; ++i) {
    const v4u u = *(const v4u*)(P.x + (size_t)row * D_ + i * 128 + li * 8);
#pragma unroll
    for (int j = 0; j < 4; ++j) { v[i * 8 + 2 * j] = lo16(u[j]); v[i * 8 + 2 * j + 1] = hi16(u[j]); }
  }
  ln32(v);
#pragma unroll
  for (int i = 0; i < 4; ++i)
    st16(wr, O_XN + (unsigned)(row * D_ + i * 128 + li * 8) * 2u, (v4u){pk2(v[i * 8], v[i * 8 + 1]), pk2(v[i * 8 + 2], v[i * 8 + 3]), pk2(v[i * 8 + 4], v[i * 8 + 5]), pk2(v[i * 8 + 6], v[i * 8 + 7])});
  ln32(v);
#pragma unroll
  for (int i = 0; i < 4; ++i)
    st16(wr, O_Y + (unsigned)(row * D_ + i * 128 + li * 8) * 2u, (v4u){pk2(v[i * 8], v[i * 8 + 1]), pk2(v[i * 8 + 2], v[i * 8 + 3]), pk2(v[i * 8 + 4], v[i * 8 + 5]), pk2(v[i * 8 + 6], v[i * 8 + 7])});
  waitvm0();
  bar();
  if (wave == 0 && lane == 0) set_flag(fr, FL_LN + half_seg, 1u, tag);
}

enum { T_K = 0, T_V = 1, T_Q = 2, T_M1 = 3, T_M2 = 4 };
__device__ __forceinline__ float gelu_tanh(float x) {
  const float p = __builtin_fmaf(x * x, -0.1029432397f, -2.3022081983f);
  const float e = __builtin_amdgcn_exp2f(x * p);
  return x * __builtin_amdgcn_rcpf(1.f + e);
}
__device__ __forceinline__ v4u pack_pair(unsigned a0, unsigned a1, unsigned b0, unsigned b1) {
  auto r0 = __builtin_amdgcn_permlane16_swap(a0, b0, false, false);
  auto r1 = __builtin_amdgcn_permlane16_swap(a1, b1, false, false);
  return (v4u){r0[0], r1[0], r0[1], r1[1]};
}
template <int T>
__device__ __forceinline__ void role_ws(const Params& P, rsrc_t wr, rsrc_t fr, unsigned tag, LAS char* lds, int idx, int wave, int lane, bool& dead) {
  constexpr int KS = T == T_M2 ? 14 : 16;
  STAMP_DECL;
  const int g = lane >> 4, c = lane & 15;
  const int kq = idx >> 2, nb = idx & 3;
  v4u w[2][KS];
  {
    const __bf16* src; size_t ld;
    if (T == T_K) { src = P.Wk + idx * 128; ld = 4096; }
    else if (T == T_V) { src = P.Wv + idx * 128; ld = 4096; }
    else if (T == T_Q) { src = P.Wq + idx * 128; ld = 4096; }
    else if (T == T_M1) { src = P.w1 + idx * 128; ld = F_; }
    else { src = P.w2 + (size_t)kq * 448 * D_ + nb * 128; ld = D_; }
    stage_rows(src, ld, KS * 32, 256, lds, wave, lane);
    const LAS char* b = lds + (8 * g + (c >> 2)) * 256 + (32 * wave + 4 * (c & 3)) * 2;
#pragma unroll
    for (int e = 0; e < 2; ++e)
#pragma unroll
      for (int u = 0; u < KS; ++u) { const v2u a = ldstr(b + u * 8192 + e * 32), d = ldstr(b + u * 8192 + 1024 + e * 32); w[e][u] = (v4u){a[0], a[1], d[0], d[1]}; }
    asm volatile("s_waitcnt lgkmcnt(0)" ::: "memory");
#pragma unroll
    for (int e = 0; e < 2; ++e)
#pragma unroll
      for (int u = 0; u < KS; ++u) asm volatile("" : "+v"(w[e][u]));
    bar();
  }
  float bias[2][4] = {{0.f, 0.f, 0.f, 0.f}, {0.f, 0.f, 0.f, 0.f}};
  if (T == T_M1) {
#pragma unroll
    for (int e = 0; e < 2; ++e)
#pragma unroll
      for (int r = 0; r < 4; ++r) bias[e][r] = (float)P.b1[idx * 128 + 16 * (2 * wave + e) + 4 * g + r];
  }
  const int myflag = (T == T_K ? FL_K : T == T_V ? FL_V : T == T_Q ? FL_Q : T == T_M1 ? FL_M1 : FL_M2) + idx;
  constexpr int NL = T == T_M2 ? 7 : 8;
  v4u pre[NL];
  unsigned known = 0;
  auto ready = [&](int s) {
    if (T == T_M2) wait_count(fr, tag, FL_M1 + ((kq * 448) >> 7), 4, (unsigned)s + 1u, known, lane, dead);
    else wait_ln(fr, tag, s, known, lane, dead);
  };
  auto fetch1 = [&](int s, int i) {
    if (T == T_M2) ld16_async(pre[i], wr, O_H + (unsigned)((s * 112 + kq * 28 + wave + 4 * i) * 1024 + lane * 16));
    else ld16_async(pre[i], wr, (T == T_M1 ? O_Y : O_XN) + (unsigned)((s * 32 + wave + 4 * i) * 1024 + lane * 16));
  };
  auto arrived = [&]() {
    if (NL == 8) asm volatile("s_waitcnt vmcnt(0)" : "+v"(pre[0]), "+v"(pre[1]), "+v"(pre[2]), "+v"(pre[3]), "+v"(pre[4]), "+v"(pre[5]), "+v"(pre[6]), "+v"(pre[NL - 1]) :: "memory");
    else asm volatile("s_waitcnt vmcnt(0)" : "+v"(pre[0]), "+v"(pre[1]), "+v"(pre[2]), "+v"(pre[3]), "+v"(pre[4]), "+v"(pre[5]), "+v"(pre[6]) :: "memory");
  };
  auto put = [&](LAS char* buf) {
#pragma unroll
    for (int i = 0; i < NL; ++i) {
      if (T == T_M2) {
        const int blk = wave + 4 * i;
        const int row = 16 * (g & 1) + c, ch = 2 * blk + (g >> 1);
        *(LAS v4u*)(buf + row * 1024 + ((ch ^ (row & 15)) * 16)) = pre[i];
      } else {
        const int row = wave + 4 * i;
        *(LAS v4u*)(buf + row * 1024 + ((lane ^ (row & 15)) * 16)) = pre[i];
      }
    }
  };
  v4u outv[2];
  const int lperm = KV_PERM ? 16 * g + 8 * (c & 1) + (c >> 1) : lane;
  auto emit = [&](int s) {
#pragma unroll
    for (int e = 0; e < 2; ++e) {
      const int tile = 2 * wave + e;
      if (T == T_V) st16(wr, O_VX + (unsigned)(((s * 32 + idx) * 8 + tile) * 1024 + lperm * 16), outv[e]);
      else if (T == T_K || T == T_Q) {
        const int kt = g & 1, u = tile >> 1, gp = 2 * (tile & 1) + (g >> 1);
        const int pos = (T == T_K && KV_PERM) ? 8 * (c & 1) + (c >> 1) : c;
        st16(wr, (T == T_K ? O_KX : O_QX) + (unsigned)((s * 32 + idx) * 8192 + ((kt * 4 + u) * 64 + 16 * gp + pos) * 16), outv[e]);
      } else if (T == T_M1) st16(wr, O_H + (unsigned)((s * 112 + idx * 8 + tile) * 1024 + lane * 16), outv[e]);
      else st16(wr, O_XP + (unsigned)(((s * 36 + 32 + kq) * 32 + nb * 8 + tile) * 1024 + lane * 16), outv[e]);
    }
  };
  ready(0);
#pragma unroll
  for (int i = 0; i < NL; ++i) fetch1(0, i);
  for (int s = 0; s < NSEG; ++s) {
    LAS char* buf = lds + (s & 1) * 32768;
    arrived();
    STAMPM(16, s, 1);
    put(buf);
    STAMPM(16, s, 2);
    bar();
    if (s > 2) { if (wave == 0 && lane == 0) set_flag(fr, (unsigned)myflag, (unsigned)(s - 1), tag); }
    if (s > 2) emit(s - 1);
    STAMPM(1, s, 0);
    const bool more = s + 1 < NSEG;
    if (more) ready(s + 1);
    STAMPM(16, s, 4);
    f32x4 a[2][2];
#pragma unroll
    for (int e = 0; e < 2; ++e) { a[e][0] = (f32x4){0.f, 0.f, 0.f, 0.f}; a[e][1] = a[e][0]; }
    const LAS char* r0 = buf + c * 1024;
    v4u xf[2][2][2];
    auto rd = [&](int grp, int bi) {
#pragma unroll
      for (int k = 0; k < 2; ++k) {
        const int sw = ((4 * (2 * grp + k) + g) ^ c) * 16;
        xf[bi][k][0] = lds16(r0 + sw); xf[bi][k][1] = lds16(r0 + 16384 + sw);
      }
    };
    rd(0, 0);
#pragma unroll
    for (int grp = 0; grp < KS / 2; ++grp) {
      if (grp + 1 < KS / 2) rd(grp + 1, (grp + 1) & 1);
      if (more) {
        if (2 * grp < NL) fetch1(s + 1, 2 * grp);
        if (2 * grp + 1 < NL) fetch1(s + 1, 2 * grp + 1);
      }
      __builtin_amdgcn_sched_barrier(0);
#pragma unroll
      for (int k = 0; k < 2; ++k) {
        const int u = 2 * grp + k;
        const v4u x0 = xf[grp & 1][k][0], x1 = xf[grp & 1][k][1];
#pragma unroll
        for (int e = 0; e < 2; ++e) {
          if (T == T_V) { a[e][0] = mfma(x0, w[e][u], a[e][0]); a[e][1] = mfma(x1, w[e][u], a[e][1]); }
          else { a[e][0] = mfma(w[e][u], x0, a[e][0]); a[e][1] = mfma(w[e][u], x1, a[e][1]); }
        }
      }
      __builtin_amdgcn_sched_barrier(0);
    }
    STAMPM(16, s, 3);
#pragma unroll
    for (int e = 0; e < 2; ++e) {
      f32x4 a0 = a[e][0], a1 = a[e][1];
      if (T == T_V) {
        outv[e] = (v4u){pk2(a0[0], a0[1]), pk2(a0[2], a0[3]), pk2(a1[0], a1[1]), pk2(a1[2], a1[3])};
      } else {
        if (T == T_M1) {
#pragma unroll
          for (int r = 0; r < 4; ++r) { a0[r] = gelu_tanh(bfr(a0[r] + bias[e][r])); a1[r] = gelu_tanh(bfr(a1[r] + bias[e][r])); }
        }
        outv[e] = pack_pair(pk2(a0[0], a0[1]), pk2(a0[2], a0[3]), pk2(a1[0], a1[1]), pk2(a1[2], a1[3]));
      }
    }
    if (s <= 1) {
      emit(s);
      waitvm0();
      bar();
      if (wave == 0 && lane == 0) set_flag(fr, (unsigned)myflag, (unsigned)(s + 1), tag);
    }
  }
  emit(NSEG - 1);
  waitvm0();
  bar();
  if (wave == 0 && lane == 0) set_flag(fr, (unsigned)myflag, (unsigned)NSEG, tag);
}

__device__ __forceinline__ void role_r(const Params& P, rsrc_t wr, rsrc_t fr, unsigned epoch, unsigned tag, LAS char* lds, int ct, int grp, int wave, int lane, bool& dead) {
  STAMP_DECL;
  const int g = lane >> 4, c = lane & 15;
  const int rowi = 16 * (g & 1) + c, col = 16 * ct + 8 * (g >> 1);
  const v4u bb = *(const v4u*)(P.b2 + col);
  unsigned kM = 0, kX[2] = {0, 0};
  int it = 0;
  for (int s = grp; s < NSEG; s += 3, ++it) {
    const v4u xx = *(const v4u*)(P.x + (size_t)(s * 32 + rowi) * D_ + col);
    wait_count(fr, tag, FL_M2, 16, (unsigned)s + 1u, kM, lane, dead);
    const v4u vm = ld16(wr, O_XP + (unsigned)(((s * 36 + 32 + wave) * 32 + ct) * 1024 + lane * 16));
    if (s & 1) wait_count(fr, tag, FL_X + 32, 32, (unsigned)(s >> 1) + 1u, kX[1], lane, dead);
    else wait_count(fr, tag, FL_X, 32, (unsigned)(s >> 1) + 1u, kX[0], lane, dead);
    if (s == NSEG - 1 && ct == 0 && wave == 0 && lane == 0) st4(fr, FL_TAG * 128u, epoch + TAG_STEP);
    v4u v[8];
#pragma unroll
    for (int i = 0; i < 8; ++i) v[i] = ld16(wr, O_XP + (unsigned)(((s * 36 + wave + 4 * i) * 32 + ct) * 1024 + lane * 16));
    float a[8] = {0.f, 0.f, 0.f, 0.f, 0.f, 0.f, 0.f, 0.f};
#pragma unroll
    for (int i = 0; i < 8; ++i)
#pragma unroll
      for (int j = 0; j < 4; ++j) { a[2 * j] += lo16(v[i][j]); a[2 * j + 1] += hi16(v[i][j]); }
    LAS float* red = (LAS float*)(lds + (it & 1) * 16384);
#pragma unroll
    for (int j = 0; j < 8; ++j) {
      red[(wave * 16 + j) * 64 + lane] = a[j];
      red[(wave * 16 + 8 + j) * 64 + lane] = (j & 1) ? hi16(vm[j >> 1]) : lo16(vm[j >> 1]);
    }
    bar();
    if (wave == (it & 3)) {
      float o[8];
#pragma unroll
      for (int j = 0; j < 8; ++j) {
        float sa = 0.f, sm = 0.f;
#pragma unroll
        for (int q = 0; q < 4; ++q) { sa += red[(q * 16 + j) * 64 + lane]; sm += red[(q * 16 + 8 + j) * 64 + lane]; }
        const float xv = (j & 1) ? hi16(xx[j >> 1]) : lo16(xx[j >> 1]);
        const float bv = (j & 1) ? hi16(bb[j >> 1]) : lo16(bb[j >> 1]);
        o[j] = bfr(xv + bfr(sa)) + bfr(bfr(sm) + bv);
      }
      *(v4u*)(P.out + (size_t)(s * 32 + rowi) * D_ + col) = (v4u){pk2(o[0], o[1]), pk2(o[2], o[3]), pk2(o[4], o[5]), pk2(o[6], o[7])};
      STAMPM(4, s, 7);
    }
  }
}

__device__ __forceinline__ void role_x(const Params& P, rsrc_t wr, rsrc_t fr, unsigned tag, LAS char* lds, int h, int par, int wave, int lane, bool& dead) {
  STAMP_DECL;
  const int g = lane >> 4, c = lane & 15;
  v4u wo[8][4];
  {
    stage_rows(P.Wo + (size_t)h * 128 * D_, D_, 128, 1024, lds, wave, lane);
    const LAS char* b = lds + (8 * g + (c >> 2)) * 1024 + (128 * wave + 4 * (c & 3)) * 2;
#pragma unroll
    for (int t = 0; t < 8; ++t)
#pragma unroll
      for (int u = 0; u < 4; ++u) { const v2u a = ldstr(b + u * 32768 + t * 32), d = ldstr(b + u * 32768 + 4096 + t * 32); wo[t][u] = (v4u){a[0], a[1], d[0], d[1]}; }
    asm volatile("s_waitcnt lgkmcnt(0)" ::: "memory");
#pragma unroll
    for (int t = 0; t < 8; ++t)
#pragma unroll
      for (int u = 0; u < 4; ++u) asm volatile("" : "+v"(wo[t][u]));
    bar();
  }
  LAS char* LKM = lds + 49152; LAS char* LVM = lds + 51200; LAS char* LO = lds + 53248;
  const int qt = wave & 1, tp = wave >> 1;
  const int lperm = KV_PERM ? 16 * g + 8 * (c & 1) + (c >> 1) : lane;
  const float c1 = 0.08838834764831845f * 1.4426950408889634f;
  v4u pq[2], pk[2], pv[2];
  unsigned kK = 0, kV = 0, kQ = 0, kC = 0;
  auto fetch = [&](int s) {
    wait_count(fr, tag, FL_K + h, 1, (unsigned)s + 1u, kK, lane, dead);
    wait_count(fr, tag, FL_V + h, 1, (unsigned)s + 1u, kV, lane, dead);
    wait_count(fr, tag, FL_Q + h, 1, (unsigned)s + 1u, kQ, lane, dead);
    const unsigned sh = (unsigned)(s * 32 + h);
#pragma unroll
    for (int i = 0; i < 2; ++i) {
      pq[i] = ld16(wr, O_QX + sh * 8192u + (2 * wave + i) * 1024 + lane * 16);
      pk[i] = ld16(wr, O_KX + sh * 8192u + (2 * wave + i) * 1024 + lane * 16);
      pv[i] = ld16(wr, O_VX + sh * 8192u + (2 * wave + i) * 1024 + lane * 16);
    }
  };
  fetch(par);
  for (int s = par; s < NSEG; s += 2) {
    LAS char* LQ = lds + ((s >> 1) & 1) * 24576; LAS char* LK = LQ + 8192; LAS char* LV = LQ + 16384;
    const unsigned sh = (unsigned)(s * 32 + h);
#pragma unroll
    for (int i = 0; i < 2; ++i) {
      *(LAS v4u*)(LQ + (2 * wave + i) * 1024 + lane * 16) = pq[i];
      *(LAS v4u*)(LK + (2 * wave + i) * 1024 + lane * 16) = pk[i];
      *(LAS v4u*)(LV + (2 * wave + i) * 1024 + lane * 16) = pv[i];
    }
    waitvm0();
    bar();
    if (s > 1 && wave == 0 && lane == 0) set_flag(fr, FL_X + 32 * par + h, (unsigned)(s >> 1), tag);
    STAMPM(2, s, 1);
    f32x4 s0 = {0.f, 0.f, 0.f, 0.f}, s1 = s0, sm = s0;
    v4u qf[4], vxf[4];
    {
      v4u k0f[4], k1f[4];
#pragma unroll
      for (int u = 0; u < 4; ++u) {
        qf[u] = lds16(LQ + (qt * 4 + u) * 1024 + lane * 16);
        k0f[u] = lds16(LK + u * 1024 + lperm * 16); k1f[u] = lds16(LK + (4 + u) * 1024 + lperm * 16);
      }
#pragma unroll
      for (int e = 0; e < 4; ++e) vxf[e] = lds16(LV + (4 * tp + e) * 1024 + lperm * 16);
      __builtin_amdgcn_sched_barrier(0);
#pragma unroll
      for (int u = 0; u < 4; ++u) { s0 = mfma(k0f[u], qf[u], s0); s1 = mfma(k1f[u], qf[u], s1); }
    }
    if (s + 2 < NSEG) fetch(s + 2);
    wait_count(fr, tag, FL_C + 2 * h, 2, (unsigned)s + 1u, kC, lane, dead);
    const v4u lm = ld16(wr, (wave < 2 ? O_KM : O_VM) + sh * 2048u + (wave & 1) * 1024 + lane * 16);
    *(LAS v4u*)(LKM + wave * 1024 + lane * 16) = lm;
    bar();
    STAMPM(2, s, 0);
    v2u vmf[4];
    {
      v4u kmf[4];
#pragma unroll
      for (int u = 0; u < 4; ++u) {
        const LAS char* kb = LKM + (32 * u + 8 * g + (c >> 2)) * 16 + (c & 1) * 8;
        const v2u ma = ldstr(kb), mb = ldstr(kb + 64);
        kmf[u] = (v4u){ma[0], ma[1], mb[0], mb[1]};
      }
#pragma unroll
      for (int e = 0; e < 4; ++e) vmf[e] = *(const LAS v2u*)(LVM + (16 * (4 * tp + e) + c) * 16 + (g & 1) * 8);
      __builtin_amdgcn_sched_barrier(0);
#pragma unroll
      for (int u = 0; u < 4; ++u) sm = mfma(kmf[u], qf[u], sm);
    }
    const int qi = 16 * qt + c;
    float x[12];
#pragma unroll
    for (int r = 0; r < 4; ++r) {
      x[r] = (4 * g + r <= qi) ? s0[r] : -INFINITY;
      x[4 + r] = (16 + 4 * g + r <= qi) ? s1[r] : -INFINITY;
      x[8 + r] = (g < 2) ? sm[r] : -INFINITY;
    }
    float mx = x[0];
#pragma unroll
    for (int i = 1; i < 12; ++i) mx = fmaxf(mx, x[i]);
    mx = xmax32(xmax16(mx));
    float l = 0.f;
#pragma unroll
    for (int i = 0; i < 12; ++i) { x[i] = __builtin_amdgcn_exp2f((x[i] - mx) * c1); l += x[i]; }
    l = xsum32(xsum16(l));
    const float rl = 1.f / l;
    const v4u pB = {pk2(x[0] * rl, x[1] * rl), pk2(x[2] * rl, x[3] * rl), pk2(x[4] * rl, x[5] * rl), pk2(x[6] * rl, x[7] * rl)};
    const v4u pM = {pk2(x[8] * rl, x[9] * rl), pk2(x[10] * rl, x[11] * rl), 0u, 0u};
#pragma unroll
    for (int e = 0; e < 4; ++e) {
      const int tau = 4 * tp + e;
      const v4u vx = vxf[e];
      v2u vm = vmf[e];
      if (g >= 2) vm = (v2u){0u, 0u};
      f32x4 o = {0.f, 0.f, 0.f, 0.f};
      o = mfma(vx, pB, o);
      o = mfma((v4u){vm[0], vm[1], 0u, 0u}, pM, o);
      const int ch = 2 * tau + (g >> 1);
      *(LAS v2u*)(LO + qi * 256 + ((ch ^ (qi & 15)) * 16) + (g & 1) * 8) = (v2u){pk2(o[0], o[1]), pk2(o[2], o[3])};
    }
    bar();
    f32x4 acc[8][2];
#pragma unroll
    for (int t = 0; t < 8; ++t) { acc[t][0] = (f32x4){0.f, 0.f, 0.f, 0.f}; acc[t][1] = acc[t][0]; }
    v4u of[4][2];
#pragma unroll
    for (int u = 0; u < 4; ++u) {
      const int sw = ((4 * u + g) ^ c) * 16;
      of[u][0] = lds16(LO + c * 256 + sw); of[u][1] = lds16(LO + (16 + c) * 256 + sw);
    }
    __builtin_amdgcn_sched_barrier(0);
#pragma unroll
    for (int u = 0; u < 4; ++u)
#pragma unroll
      for (int t = 0; t < 8; ++t) { acc[t][0] = mfma(wo[t][u], of[u][0], acc[t][0]); acc[t][1] = mfma(wo[t][u], of[u][1], acc[t][1]); }
#pragma unroll
    for (int t = 0; t < 8; ++t) {
      const v4u o = pack_pair(pk2(acc[t][0][0], acc[t][0][1]), pk2(acc[t][0][2], acc[t][0][3]), pk2(acc[t][1][0], acc[t][1][1]), pk2(acc[t][1][2], acc[t][1][3]));
      st16(wr, O_XP + (unsigned)(((s * 36 + h) * 32 + 8 * wave + t) * 1024 + lane * 16), o);
    }
  }
  waitvm0();
  bar();
  if (wave == 0 && lane == 0) set_flag(fr, FL_X + 32 * par + h, (unsigned)(NSEG / 2), tag);
}

__device__ __forceinline__ unsigned timed_load(const char* p) {
  unsigned long long t0, t1; unsigned v;
  asm volatile("s_memtime %0\n\ts_waitcnt lgkmcnt(0)\n\tglobal_load_dword %2, %3, off sc1\n\ts_waitcnt vmcnt(0)\n\ts_memtime %1\n\ts_waitcnt lgkmcnt(0)"
               : "=&s"(t0), "=&s"(t1), "=&v"(v) : "v"(p) : "memory");
  return (unsigned)(t1 - t0);
}
__device__ __forceinline__ void init_sentinels(rsrc_t wr, rsrc_t fr, unsigned tag, int p, int wave, int lane) {
  const v4u sent = {0xFFFFFFFFu, 0xFFFFFFFFu, 0xFFFFFFFFu, 0xFFFFFFFFu};
  for (int i = wave; i < 64; i += 4) {
    const int e = i * 8 + (lane >> 3);
    st16(wr, O_MEM + (unsigned)(e * 8192 + p * 128 + (lane & 7) * 16), sent);
  }
  for (int i = wave; i < 32; i += 4) st16(wr, O_PART + (unsigned)(((i >> 4) * 64 + p) * 16384 + (i & 15) * 1024 + lane * 16), sent);
  waitvm0();
  bar();
  if (wave == 0 && lane == 0) set_flag(fr, FL_BAR + p, 1u, tag);
}
__device__ __forceinline__ void touch_pages(const Params& P, rsrc_t wr, int wave, int lane) {
  if (wave != 1) return;
  unsigned a = ld4(wr, (unsigned)lane * 2 * MB);
  unsigned b = lane < O_END / (2 * MB) - 64 ? ld4(wr, (unsigned)(lane + 64) * 2 * MB) : 0u;
  unsigned o = lane == 0 ? *(const GAS unsigned*)(P.out + 1024 * 512) : 0u;
  asm volatile("" :: "v"(a), "v"(b), "v"(o));
}
__device__ __forceinline__ void start_delay(int n) {
#pragma unroll 1
  for (int i = 0; i < n; ++i) __builtin_amdgcn_s_sleep(32);
}

__device__ __forceinline__ void role_chain(const Params& P, rsrc_t wr, rsrc_t fr, int h, int a, int xg, int wave, int lane) {
  const __bf16* pwq = P.Wq + h * 128; const __bf16* pwk = P.Wk + h * 128; const __bf16* pwv = P.Wv + h * 128 + 64 * a;
  const __bf16* pwo = P.Wo + (size_t)(h * 128 + 64 * a) * D_;

  unsigned tid = threadIdx.x;
  asm volatile(
      "v_lshrrev_b32 v1, 6, v0\n"
      "s_nop 0\n"
      "v_readfirstlane_b32 s40, v1\n"
      "s_mov_b32 s42, 0\n"
      "s_mov_b32 s55, 0\n"
      "s_mov_b32 s56, 0x3e0293ee\n"
      "s_lshl_b32 s57, %[h], 1\n"
      "s_add_u32 s57, s57, %[a]\n"
      "s_mov_b32 s62, 0xff00ff00\n"
      "s_mov_b32 s63, 0xff00ff00\n"
      "s_mov_b32 s64, 0xaaaaaaaa\n"
      "s_mov_b32 s65, 0xaaaaaaaa\n"
      "s_mov_b32 s74, 0x30003\n"
      "s_mov_b32 s75, 0x30003\n"
      "s_mov_b32 s76, 0xc000c\n"
      "s_mov_b32 s77, 0xc000c\n"
      "s_mov_b32 s78, 0x300030\n"
      "s_mov_b32 s79, 0x300030\n"
      "s_mov_b32 s80, 0xc000c0\n"
      "s_mov_b32 s81, 0xc000c0\n"
      "v_and_b32 v212, 63, v0\n"
      "v_and_b32 v213, 15, v212\n"
      "v_lshrrev_b32 v214, 4, v212\n"
      "v_lshlrev_b32 v215, 8, v214\n"
      "v_and_b32 v216, 1, v213\n"
      "v_lshlrev_b32 v216, 7, v216\n"
      "v_add_u32 v215, v215, v216\n"
      "v_lshrrev_b32 v216, 1, v213\n"
      "v_lshlrev_b32 v216, 4, v216\n"
      "v_add_u32 v255, v215, v216\n"
      "v_lshlrev_b32 v215, 4, v212\n"
      "s_lshl_b32 s43, s40, 11\n"
      "v_add_u32 v254, s43, v215\n"
      "v_and_b32 v215, 1, v212\n"
      "v_mul_u32_u24 v215, 0xaaaa, v215\n"
      "v_add_u32 v253, 0x4444, v215\n"
      "v_lshlrev_b32 v215, 4, v213\n"
      "v_lshlrev_b32 v216, 2, v214\n"
      "s_lshl_b32 s43, s40, 9\n"
      "s_add_u32 s43, s43, 8192\n"
      "v_add3_u32 v252, v215, v216, s43\n"
      "v_lshlrev_b32 v215, 5, v213\n"
      "v_lshlrev_b32 v216, 3, v214\n"
      "s_lshl_b32 s43, s40, 10\n"
      "s_add_u32 s43, s43, 10240\n"
      "v_add3_u32 v251, v215, v216, s43\n"
      "v_lshlrev_b32 v215, 3, v214\n"
      "v_lshrrev_b32 v216, 2, v213\n"
      "v_add_u32 v215, v215, v216\n"
      "v_lshlrev_b32 v250, 4, v215\n"
      "v_and_b32 v216, 1, v213\n"
      "v_lshlrev_b32 v216, 3, v216\n"
      "s_mov_b32 s43, 8192\n"
      "v_add3_u32 v250, v250, v216, s43\n"
      "v_lshlrev_b32 v249, 5, v215\n"
      "v_and_b32 v216, 3, v213\n"
      "v_lshlrev_b32 v216, 3, v216\n"
      "s_mov_b32 s43, 10240\n"
      "v_add3_u32 v249, v249, v216, s43\n"
      "v_lshrrev_b32 v215, 1, v214\n"
      "v_lshlrev_b32 v215, 7, v215\n"
      "v_lshlrev_b32 v216, 4, v213\n"
      "v_add_u32 v215, v215, v216\n"
      "v_and_b32 v216, 1, v214\n"
      "v_lshlrev_b32 v216, 3, v216\n"
      "s_lshl_b32 s43, s40, 8\n"
      "s_add_u32 s43, s43, 14336\n"
      "v_add3_u32 v248, v215, v216, s43\n"
      "v_lshlrev_b32 v215, 3, v212\n"
      "v_add_u32 v215, 15360, v215\n"
      "v_cndmask_b32 v248, v248, v215, s[62:63]\n"
      "v_lshrrev_b32 v215, 1, v213\n"
      "s_lshl_b32 s43, s40, 4\n"
      "v_add_u32 v215, s43, v215\n"
      "v_lshlrev_b32 v247, 14, v215\n"
      "v_lshlrev_b32 v215, 1, v214\n"
      "v_and_b32 v216, 1, v213\n"
      "v_add_u32 v215, v215, v216\n"
      "v_lshlrev_b32 v215, 4, v215\n"
      "v_and_b32 v216, 14, v213\n"
      "s_add_u32 s43, s57, s40\n"
      "v_add_u32 v216, s43, v216\n"
      "v_and_b32 v216, 63, v216\n"
      "v_lshlrev_b32 v216, 7, v216\n"
      "v_add3_u32 v247, v247, v215, v216\n"
      "v_mov_b32 v245, 0\n"
      "buffer_load_dword v243, v245, %[fr], 0 offen sc1\n"
      "v_lshrrev_b32 v215, 4, v212\n"
      "s_lshl_b32 s43, s40, 2\n"
      "v_add_u32 v215, s43, v215\n"
      "v_lshlrev_b32 v215, 13, v215\n"
      "v_lshlrev_b32 v216, 4, v213\n"
      "v_add_u32 v217, v215, v216\n"
      "v_lshrrev_b32 v215, 3, v212\n"
      "s_lshl_b32 s43, s40, 3\n"
      "v_add_u32 v215, s43, v215\n"
      "v_lshlrev_b32 v215, 13, v215\n"
      "v_and_b32 v216, 7, v212\n"
      "v_lshlrev_b32 v216, 4, v216\n"
      "v_add_u32 v218, v215, v216\n"
      "v_lshlrev_b32 v219, 4, v212\n"
      "s_lshl_b32 s43, s40, 10\n"
      "v_add_u32 v219, s43, v219\n"
      "v_and_b32 v215, 1, v214\n"
      "v_lshlrev_b32 v224, 5, v215\n"
      "v_lshrrev_b32 v215, 1, v214\n"
      "v_lshlrev_b32 v215, 2, v215\n"
      "v_add_u32 v224, v224, v215\n"
      "v_lshrrev_b32 v215, 2, v213\n"
      "v_add_u32 v224, v224, v215\n"
      "v_and_b32 v216, 3, v213\n"
      "v_lshlrev_b32 v215, 8, v224\n"
      "v_lshlrev_b32 v220, 3, v216\n"
      "s_lshl_b32 s43, s40, 6\n"
      "v_add3_u32 v220, v220, v215, s43\n"
      "v_lshlrev_b32 v215, 7, v224\n"
      "v_lshlrev_b32 v221, 3, v216\n"
      "s_lshl_b32 s43, s40, 5\n"
      "v_add3_u32 v221, v221, v215, s43\n"
      "v_lshrrev_b32 v215, 1, v214\n"
      "v_lshlrev_b32 v224, 4, v215\n"
      "v_and_b32 v215, 1, v214\n"
      "v_lshlrev_b32 v215, 2, v215\n"
      "v_add_u32 v224, v224, v215\n"
      "v_lshrrev_b32 v215, 3, v213\n"
      "v_lshlrev_b32 v215, 3, v215\n"
      "v_add_u32 v224, v224, v215\n"
      "v_lshrrev_b32 v215, 2, v213\n"
      "v_and_b32 v215, 1, v215\n"
      "v_add_u32 v224, v224, v215\n"
      "v_lshlrev_b32 v215, 10, v224\n"
      "v_lshlrev_b32 v222, 3, v216\n"
      "s_lshl_b32 s43, s40, 8\n"
      "v_add3_u32 v222, v222, v215, s43\n"
      "s_mov_b64 s[66:67], %[pwq]\n"
      "s_lshl_b32 s68, s40, 10\n"
      "s_add_u32 s68, s68, 0\n"
      "s_mov_b32 m0, s68\n"
      "s_add_u32 s68, s68, 4096\n"
      "global_load_lds_dwordx4 v217, s[66:67]\n"
      "s_add_u32 s66, s66, 131072\n"
      "s_addc_u32 s67, s67, 0\n"
      "s_mov_b32 m0, s68\n"
      "s_add_u32 s68, s68, 4096\n"
      "global_load_lds_dwordx4 v217, s[66:67]\n"
      "s_add_u32 s66, s66, 131072\n"
      "s_addc_u32 s67, s67, 0\n"
      "s_mov_b32 m0, s68\n"
      "s_add_u32 s68, s68, 4096\n"
      "global_load_lds_dwordx4 v217, s[66:67]\n"
      "s_add_u32 s66, s66, 131072\n"
      "s_addc_u32 s67, s67, 0\n"
      "s_mov_b32 m0, s68\n"
      "s_add_u32 s68, s68, 4096\n"
      "global_load_lds_dwordx4 v217, s[66:67]\n"
      "s_add_u32 s66, s66, 131072\n"
      "s_addc_u32 s67, s67, 0\n"
      "s_mov_b32 m0, s68\n"
      "s_add_u32 s68, s68, 4096\n"
      "global_load_lds_dwordx4 v217, s[66:67]\n"
      "s_add_u32 s66, s66, 131072\n"
      "s_addc_u32 s67, s67, 0\n"
      "s_mov_b32 m0, s68\n"
      "s_add_u32 s68, s68, 4096\n"
      "global_load_lds_dwordx4 v217, s[66:67]\n"
      "s_add_u32 s66, s66, 131072\n"
      "s_addc_u32 s67, s67, 0\n"
      "s_mov_b32 m0, s68\n"
      "s_add_u32 s68, s68, 4096\n"
      "global_load_lds_dwordx4 v217, s[66:67]\n"
      "s_add_u32 s66, s66, 131072\n"
      "s_addc_u32 s67, s67, 0\n"
      "s_mov_b32 m0, s68\n"
      "s_add_u32 s68, s68, 4096\n"
      "global_load_lds_dwordx4 v217, s[66:67]\n"
      "s_mov_b64 s[66:67], %[pwq]\n"
      "s_add_u32 s66, s66, 1048576\n"
      "s_addc_u32 s67, s67, 0\n"
      "s_lshl_b32 s68, s40, 10\n"
      "s_add_u32 s68, s68, 32768\n"
      "s_mov_b32 m0, s68\n"
      "s_add_u32 s68, s68, 4096\n"
      "global_load_lds_dwordx4 v217, s[66:67]\n"
      "s_add_u32 s66, s66, 131072\n"
      "s_addc_u32 s67, s67, 0\n"
      "s_mov_b32 m0, s68\n"
      "s_add_u32 s68, s68, 4096\n"
      "global_load_lds_dwordx4 v217, s[66:67]\n"
      "s_add_u32 s66, s66, 131072\n"
      "s_addc_u32 s67, s67, 0\n"
      "s_mov_b32 m0, s68\n"
      "s_add_u32 s68, s68, 4096\n"
      "global_load_lds_dwordx4 v217, s[66:67]\n"
      "s_add_u32 s66, s66, 131072\n"
      "s_addc_u32 s67, s67, 0\n"
      "s_mov_b32 m0, s68\n"
      "s_add_u32 s68, s68, 4096\n"
      "global_load_lds_dwordx4 v217, s[66:67]\n"
      "s_add_u32 s66, s66, 131072\n"
      "s_addc_u32 s67, s67, 0\n"
      "s_mov_b32 m0, s68\n"
      "s_add_u32 s68, s68, 4096\n"
      "global_load_lds_dwordx4 v217, s[66:67]\n"
      "s_add_u32 s66, s66, 131072\n"
      "s_addc_u32 s67, s67, 0\n"
      "s_mov_b32 m0, s68\n"
      "s_add_u32 s68, s68, 4096\n"
      "global_load_lds_dwordx4 v217, s[66:67]\n"
      "s_add_u32 s66, s66, 131072\n"
      "s_addc_u32 s67, s67, 0\n"
      "s_mov_b32 m0, s68\n"
      "s_add_u32 s68, s68, 4096\n"
      "global_load_lds_dwordx4 v217, s[66:67]\n"
      "s_add_u32 s66, s66, 131072\n"
      "s_addc_u32 s67, s67, 0\n"
      "s_mov_b32 m0, s68\n"
      "s_add_u32 s68, s68, 4096\n"
      "global_load_lds_dwordx4 v217, s[66:67]\n"
      "s_mov_b64 s[66:67], %[pwq]\n"
      "s_add_u32 s66, s66, 2097152\n"
      "s_addc_u32 s67, s67, 0\n"
      "s_lshl_b32 s68, s40, 10\n"
      "s_add_u32 s68, s68, 65536\n"
      "s_mov_b32 m0, s68\n"
      "s_add_u32 s68, s68, 4096\n"
      "global_load_lds_dwordx4 v217, s[66:67]\n"
      "s_add_u32 s66, s66, 131072\n"
      "s_addc_u32 s67, s67, 0\n"
      "s_mov_b32 m0, s68\n"
      "s_add_u32 s68, s68, 4096\n"
      "global_load_lds_dwordx4 v217, s[66:67]\n"
      "s_add_u32 s66, s66, 131072\n"
      "s_addc_u32 s67, s67, 0\n"
      "s_mov_b32 m0, s68\n"
      "s_add_u32 s68, s68, 4096\n"
      "global_load_lds_dwordx4 v217, s[66:67]\n"
      "s_add_u32 s66, s66, 131072\n"
      "s_addc_u32 s67, s67, 0\n"
      "s_mov_b32 m0, s68\n"
      "s_add_u32 s68, s68, 4096\n"
      "global_load_lds_dwordx4 v217, s[66:67]\n"
      "s_add_u32 s66, s66, 131072\n"
      "s_addc_u32 s67, s67, 0\n"
      "s_mov_b32 m0, s68\n"
      "s_add_u32 s68, s68, 4096\n"
      "global_load_lds_dwordx4 v217, s[66:67]\n"
      "s_add_u32 s66, s66, 131072\n"
      "s_addc_u32 s67, s67, 0\n"
      "s_mov_b32 m0, s68\n"
      "s_add_u32 s68, s68, 4096\n"
      "global_load_lds_dwordx4 v217, s[66:67]\n"
      "s_add_u32 s66, s66, 131072\n"
      "s_addc_u32 s67, s67, 0\n"
      "s_mov_b32 m0, s68\n"
      "s_add_u32 s68, s68, 4096\n"
      "global_load_lds_dwordx4 v217, s[66:67]\n"
      "s_add_u32 s66, s66, 131072\n"
      "s_addc_u32 s67, s67, 0\n"
      "s_mov_b32 m0, s68\n"
      "s_add_u32 s68, s68, 4096\n"
      "global_load_lds_dwordx4 v217, s[66:67]\n"
      "s_waitcnt vmcnt(16)\n"
      "s_barrier\n"
      "v_add_u32 v223, 0, v220\n"
      "s_mov_b64 s[66:67], %[pwq]\n"
      "s_add_u32 s66, s66, 3145728\n"
      "s_addc_u32 s67, s67, 0\n"
      "s_lshl_b32 s68, s40, 10\n"
      "s_add_u32 s68, s68, 98304\n"
      "s_mov_b32 m0, s68\n"
      "s_add_u32 s68, s68, 4096\n"
      "global_load_lds_dwordx4 v217, s[66:67]\n"
      "s_add_u32 s66, s66, 131072\n"
      "s_addc_u32 s67, s67, 0\n"
      "ds_read_b64_tr_b16 a[0:1], v223 offset:0\n"
      "ds_read_b64_tr_b16 a[2:3], v223 offset:4096\n"
      "s_mov_b32 m0, s68\n"
      "s_add_u32 s68, s68, 4096\n"
      "global_load_lds_dwordx4 v217, s[66:67]\n"
      "s_add_u32 s66, s66, 131072\n"
      "s_addc_u32 s67, s67, 0\n"
      "ds_read_b64_tr_b16 a[4:5], v223 offset:2048\n"
      "ds_read_b64_tr_b16 a[6:7], v223 offset:6144\n"
      "s_mov_b32 m0, s68\n"
      "s_add_u32 s68, s68, 4096\n"
      "global_load_lds_dwordx4 v217, s[66:67]\n"
      "s_add_u32 s66, s66, 131072\n"
      "s_addc_u32 s67, s67, 0\n"
      "ds_read_b64_tr_b16 a[8:9], v223 offset:16384\n"
      "ds_read_b64_tr_b16 a[10:11], v223 offset:20480\n"
      "s_mov_b32 m0, s68\n"
      "s_add_u32 s68, s68, 4096\n"
      "global_load_lds_dwordx4 v217, s[66:67]\n"
      "s_add_u32 s66, s66, 131072\n"
      "s_addc_u32 s67, s67, 0\n"
      "ds_read_b64_tr_b16 a[12:13], v223 offset:18432\n"
      "ds_read_b64_tr_b16 a[14:15], v223 offset:22528\n"
      "s_waitcnt lgkmcnt(0)\n"
      "s_mov_b32 m0, s68\n"
      "s_add_u32 s68, s68, 4096\n"
      "global_load_lds_dwordx4 v217, s[66:67]\n"
      "s_add_u32 s66, s66, 131072\n"
      "s_addc_u32 s67, s67, 0\n"
      "ds_read_b64_tr_b16 a[64:65], v223 offset:32\n"
      "ds_read_b64_tr_b16 a[66:67], v223 offset:4128\n"
      "s_mov_b32 m0, s68\n"
      "s_add_u32 s68, s68, 4096\n"
      "global_load_lds_dwordx4 v217, s[66:67]\n"
      "s_add_u32 s66, s66, 131072\n"
      "s_addc_u32 s67, s67, 0\n"
      "ds_read_b64_tr_b16 a[68:69], v223 offset:2080\n"
      "ds_read_b64_tr_b16 a[70:71], v223 offset:6176\n"
      "s_mov_b32 m0, s68\n"
      "s_add_u32 s68, s68, 4096\n"
      "global_load_lds_dwordx4 v217, s[66:67]\n"
      "s_add_u32 s66, s66, 131072\n"
      "s_addc_u32 s67, s67, 0\n"
      "ds_read_b64_tr_b16 a[72:73], v223 offset:16416\n"
      "ds_read_b64_tr_b16 a[74:75], v223 offset:20512\n"
      "s_mov_b32 m0, s68\n"
      "s_add_u32 s68, s68, 4096\n"
      "global_load_lds_dwordx4 v217, s[66:67]\n"
      "ds_read_b64_tr_b16 a[76:77], v223 offset:18464\n"
      "ds_read_b64_tr_b16 a[78:79], v223 offset:22560\n"
      "s_waitcnt lgkmcnt(0)\n"
      "s_waitcnt lgkmcnt(0)\n"
      "s_waitcnt vmcnt(16)\n"
      "s_barrier\n"
      "v_add_u32 v223, 32768, v220\n"
      "s_mov_b64 s[66:67], %[pwk]\n"
      "s_lshl_b32 s68, s40, 10\n"
      "s_add_u32 s68, s68, 0\n"
      "s_mov_b32 m0, s68\n"
      "s_add_u32 s68, s68, 4096\n"
      "global_load_lds_dwordx4 v217, s[66:67]\n"
      "s_add_u32 s66, s66, 131072\n"
      "s_addc_u32 s67, s67, 0\n"
      "ds_read_b64_tr_b16 a[16:17], v223 offset:0\n"
      "ds_read_b64_tr_b16 a[18:19], v223 offset:4096\n"
      "s_mov_b32 m0, s68\n"
      "s_add_u32 s68, s68, 4096\n"
      "global_load_lds_dwordx4 v217, s[66:67]\n"
      "s_add_u32 s66, s66, 131072\n"
      "s_addc_u32 s67, s67, 0\n"
      "ds_read_b64_tr_b16 a[20:21], v223 offset:2048\n"
      "ds_read_b64_tr_b16 a[22:23], v223 offset:6144\n"
      "s_mov_b32 m0, s68\n"
      "s_add_u32 s68, s68, 4096\n"
      "global_load_lds_dwordx4 v217, s[66:67]\n"
      "s_add_u32 s66, s66, 131072\n"
      "s_addc_u32 s67, s67, 0\n"
      "ds_read_b64_tr_b16 a[24:25], v223 offset:16384\n"
      "ds_read_b64_tr_b16 a[26:27], v223 offset:20480\n"
      "s_mov_b32 m0, s68\n"
      "s_add_u32 s68, s68, 4096\n"
      "global_load_lds_dwordx4 v217, s[66:67]\n"
      "s_add_u32 s66, s66, 131072\n"
      "s_addc_u32 s67, s67, 0\n"
      "ds_read_b64_tr_b16 a[28:29], v223 offset:18432\n"
      "ds_read_b64_tr_b16 a[30:31], v223 offset:22528\n"
      "s_waitcnt lgkmcnt(0)\n"
      "s_mov_b32 m0, s68\n"
      "s_add_u32 s68, s68, 4096\n"
      "global_load_lds_dwordx4 v217, s[66:67]\n"
      "s_add_u32 s66, s66, 131072\n"
      "s_addc_u32 s67, s67, 0\n"
      "ds_read_b64_tr_b16 a[80:81], v223 offset:32\n"
      "ds_read_b64_tr_b16 a[82:83], v223 offset:4128\n"
      "s_mov_b32 m0, s68\n"
      "s_add_u32 s68, s68, 4096\n"
      "global_load_lds_dwordx4 v217, s[66:67]\n"
      "s_add_u32 s66, s66, 131072\n"
      "s_addc_u32 s67, s67, 0\n"
      "ds_read_b64_tr_b16 a[84:85], v223 offset:2080\n"
      "ds_read_b64_tr_b16 a[86:87], v223 offset:6176\n"
      "s_mov_b32 m0, s68\n"
      "s_add_u32 s68, s68, 4096\n"
      "global_load_lds_dwordx4 v217, s[66:67]\n"
      "s_add_u32 s66, s66, 131072\n"
      "s_addc_u32 s67, s67, 0\n"
      "ds_read_b64_tr_b16 a[88:89], v223 offset:16416\n"
      "ds_read_b64_tr_b16 a[90:91], v223 offset:20512\n"
      "s_mov_b32 m0, s68\n"
      "s_add_u32 s68, s68, 4096\n"
      "global_load_lds_dwordx4 v217, s[66:67]\n"
      "ds_read_b64_tr_b16 a[92:93], v223 offset:18464\n"
      "ds_read_b64_tr_b16 a[94:95], v223 offset:22560\n"
      "s_waitcnt lgkmcnt(0)\n"
      "s_waitcnt lgkmcnt(0)\n"
      "s_waitcnt vmcnt(16)\n"
      "s_barrier\n"
      "v_add_u32 v223, 65536, v220\n"
      "s_mov_b64 s[66:67], %[pwk]\n"
      "s_add_u32 s66, s66, 1048576\n"
      "s_addc_u32 s67, s67, 0\n"
      "s_lshl_b32 s68, s40, 10\n"
      "s_add_u32 s68, s68, 32768\n"
      "s_mov_b32 m0, s68\n"
      "s_add_u32 s68, s68, 4096\n"
      "global_load_lds_dwordx4 v217, s[66:67]\n"
      "s_add_u32 s66, s66, 131072\n"
      "s_addc_u32 s67, s67, 0\n"
      "ds_read_b64_tr_b16 a[32:33], v223 offset:0\n"
      "ds_read_b64_tr_b16 a[34:35], v223 offset:4096\n"
      "s_mov_b32 m0, s68\n"
      "s_add_u32 s68, s68, 4096\n"
      "global_load_lds_dwordx4 v217, s[66:67]\n"
      "s_add_u32 s66, s66, 131072\n"
      "s_addc_u32 s67, s67, 0\n"
      "ds_read_b64_tr_b16 a[36:37], v223 offset:2048\n"
      "ds_read_b64_tr_b16 a[38:39], v223 offset:6144\n"
      "s_mov_b32 m0, s68\n"
      "s_add_u32 s68, s68, 4096\n"
      "global_load_lds_dwordx4 v217, s[66:67]\n"
      "s_add_u32 s66, s66, 131072\n"
      "s_addc_u32 s67, s67, 0\n"
      "ds_read_b64_tr_b16 a[40:41], v223 offset:16384\n"
      "ds_read_b64_tr_b16 a[42:43], v223 offset:20480\n"
      "s_mov_b32 m0, s68\n"
      "s_add_u32 s68, s68, 4096\n"
      "global_load_lds_dwordx4 v217, s[66:67]\n"
      "s_add_u32 s66, s66, 131072\n"
      "s_addc_u32 s67, s67, 0\n"
      "ds_read_b64_tr_b16 a[44:45], v223 offset:18432\n"
      "ds_read_b64_tr_b16 a[46:47], v223 offset:22528\n"
      "s_waitcnt lgkmcnt(0)\n"
      "s_mov_b32 m0, s68\n"
      "s_add_u32 s68, s68, 4096\n"
      "global_load_lds_dwordx4 v217, s[66:67]\n"
      "s_add_u32 s66, s66, 131072\n"
      "s_addc_u32 s67, s67, 0\n"
      "ds_read_b64_tr_b16 a[96:97], v223 offset:32\n"
      "ds_read_b64_tr_b16 a[98:99], v223 offset:4128\n"
      "s_mov_b32 m0, s68\n"
      "s_add_u32 s68, s68, 4096\n"
      "global_load_lds_dwordx4 v217, s[66:67]\n"
      "s_add_u32 s66, s66, 131072\n"
      "s_addc_u32 s67, s67, 0\n"
      "ds_read_b64_tr_b16 a[100:101], v223 offset:2080\n"
      "ds_read_b64_tr_b16 a[102:103], v223 offset:6176\n"
      "s_mov_b32 m0, s68\n"
      "s_add_u32 s68, s68, 4096\n"
      "global_load_lds_dwordx4 v217, s[66:67]\n"
      "s_add_u32 s66, s66, 131072\n"
      "s_addc_u32 s67, s67, 0\n"
      "ds_read_b64_tr_b16 a[104:105], v223 offset:16416\n"
      "ds_read_b64_tr_b16 a[106:107], v223 offset:20512\n"
      "s_mov_b32 m0, s68\n"
      "s_add_u32 s68, s68, 4096\n"
      "global_load_lds_dwordx4 v217, s[66:67]\n"
      "ds_read_b64_tr_b16 a[108:109], v223 offset:18464\n"
      "ds_read_b64_tr_b16 a[110:111], v223 offset:22560\n"
      "s_waitcnt lgkmcnt(0)\n"
      "s_waitcnt lgkmcnt(0)\n"
      "s_waitcnt vmcnt(16)\n"
      "s_barrier\n"
      "v_add_u32 v223, 98304, v220\n"
      "s_mov_b64 s[66:67], %[pwk]\n"
      "s_add_u32 s66, s66, 2097152\n"
      "s_addc_u32 s67, s67, 0\n"
      "s_lshl_b32 s68, s40, 10\n"
      "s_add_u32 s68, s68, 65536\n"
      "s_mov_b32 m0, s68\n"
      "s_add_u32 s68, s68, 4096\n"
      "global_load_lds_dwordx4 v217, s[66:67]\n"
      "s_add_u32 s66, s66, 131072\n"
      "s_addc_u32 s67, s67, 0\n"
      "ds_read_b64_tr_b16 a[48:49], v223 offset:0\n"
      "ds_read_b64_tr_b16 a[50:51], v223 offset:4096\n"
      "s_mov_b32 m0, s68\n"
      "s_add_u32 s68, s68, 4096\n"
      "global_load_lds_dwordx4 v217, s[66:67]\n"
      "s_add_u32 s66, s66, 131072\n"
      "s_addc_u32 s67, s67, 0\n"
      "ds_read_b64_tr_b16 a[52:53], v223 offset:2048\n"
      "ds_read_b64_tr_b16 a[54:55], v223 offset:6144\n"
      "s_mov_b32 m0, s68\n"
      "s_add_u32 s68, s68, 4096\n"
      "global_load_lds_dwordx4 v217, s[66:67]\n"
      "s_add_u32 s66, s66, 131072\n"
      "s_addc_u32 s67, s67, 0\n"
      "ds_read_b64_tr_b16 a[56:57], v223 offset:16384\n"
      "ds_read_b64_tr_b16 a[58:59], v223 offset:20480\n"
      "s_mov_b32 m0, s68\n"
      "s_add_u32 s68, s68, 4096\n"
      "global_load_lds_dwordx4 v217, s[66:67]\n"
      "s_add_u32 s66, s66, 131072\n"
      "s_addc_u32 s67, s67, 0\n"
      "ds_read_b64_tr_b16 a[60:61], v223 offset:18432\n"
      "ds_read_b64_tr_b16 a[62:63], v223 offset:22528\n"
      "s_waitcnt lgkmcnt(0)\n"
      "s_mov_b32 m0, s68\n"
      "s_add_u32 s68, s68, 4096\n"
      "global_load_lds_dwordx4 v217, s[66:67]\n"
      "s_add_u32 s66, s66, 131072\n"
      "s_addc_u32 s67, s67, 0\n"
      "ds_read_b64_tr_b16 a[112:113], v223 offset:32\n"
      "ds_read_b64_tr_b16 a[114:115], v223 offset:4128\n"
      "s_mov_b32 m0, s68\n"
      "s_add_u32 s68, s68, 4096\n"
      "global_load_lds_dwordx4 v217, s[66:67]\n"
      "s_add_u32 s66, s66, 131072\n"
      "s_addc_u32 s67, s67, 0\n"
      "ds_read_b64_tr_b16 a[116:117], v223 offset:2080\n"
      "ds_read_b64_tr_b16 a[118:119], v223 offset:6176\n"
      "s_mov_b32 m0, s68\n"
      "s_add_u32 s68, s68, 4096\n"
      "global_load_lds_dwordx4 v217, s[66:67]\n"
      "s_add_u32 s66, s66, 131072\n"
      "s_addc_u32 s67, s67, 0\n"
      "ds_read_b64_tr_b16 a[120:121], v223 offset:16416\n"
      "ds_read_b64_tr_b16 a[122:123], v223 offset:20512\n"
      "s_mov_b32 m0, s68\n"
      "s_add_u32 s68, s68, 4096\n"
      "global_load_lds_dwordx4 v217, s[66:67]\n"
      "ds_read_b64_tr_b16 a[124:125], v223 offset:18464\n"
      "ds_read_b64_tr_b16 a[126:127], v223 offset:22560\n"
      "s_waitcnt lgkmcnt(0)\n"
      "s_waitcnt lgkmcnt(0)\n"
      "s_waitcnt vmcnt(16)\n"
      "s_barrier\n"
      "v_add_u32 v223, 0, v220\n"
      "s_mov_b64 s[66:67], %[pwk]\n"
      "s_add_u32 s66, s66, 3145728\n"
      "s_addc_u32 s67, s67, 0\n"
      "s_lshl_b32 s68, s40, 10\n"
      "s_add_u32 s68, s68, 98304\n"
      "s_mov_b32 m0, s68\n"
      "s_add_u32 s68, s68, 4096\n"
      "global_load_lds_dwordx4 v217, s[66:67]\n"
      "s_add_u32 s66, s66, 131072\n"
      "s_addc_u32 s67, s67, 0\n"
      "ds_read_b64_tr_b16 a[128:129], v223 offset:0\n"
      "ds_read_b64_tr_b16 a[130:131], v223 offset:4096\n"
      "s_mov_b32 m0, s68\n"
      "s_add_u32 s68, s68, 4096\n"
      "global_load_lds_dwordx4 v217, s[66:67]\n"
      "s_add_u32 s66, s66, 131072\n"
      "s_addc_u32 s67, s67, 0\n"
      "ds_read_b64_tr_b16 a[132:133], v223 offset:2048\n"
      "ds_read_b64_tr_b16 a[134:135], v223 offset:6144\n"
      "s_mov_b32 m0, s68\n"
      "s_add_u32 s68, s68, 4096\n"
      "global_load_lds_dwordx4 v217, s[66:67]\n"
      "s_add_u32 s66, s66, 131072\n"
      "s_addc_u32 s67, s67, 0\n"
      "ds_read_b64_tr_b16 a[136:137], v223 offset:16384\n"
      "ds_read_b64_tr_b16 a[138:139], v223 offset:20480\n"
      "s_mov_b32 m0, s68\n"
      "s_add_u32 s68, s68, 4096\n"
      "global_load_lds_dwordx4 v217, s[66:67]\n"
      "s_add_u32 s66, s66, 131072\n"
      "s_addc_u32 s67, s67, 0\n"
      "ds_read_b64_tr_b16 a[140:141], v223 offset:18432\n"
      "ds_read_b64_tr_b16 a[142:143], v223 offset:22528\n"
      "s_waitcnt lgkmcnt(0)\n"
      "s_mov_b32 m0, s68\n"
      "s_add_u32 s68, s68, 4096\n"
      "global_load_lds_dwordx4 v217, s[66:67]\n"
      "s_add_u32 s66, s66, 131072\n"
      "s_addc_u32 s67, s67, 0\n"
      "ds_read_b64_tr_b16 a[192:193], v223 offset:32\n"
      "ds_read_b64_tr_b16 a[194:195], v223 offset:4128\n"
      "s_mov_b32 m0, s68\n"
      "s_add_u32 s68, s68, 4096\n"
      "global_load_lds_dwordx4 v217, s[66:67]\n"
      "s_add_u32 s66, s66, 131072\n"
      "s_addc_u32 s67, s67, 0\n"
      "ds_read_b64_tr_b16 a[196:197], v223 offset:2080\n"
      "ds_read_b64_tr_b16 a[198:199], v223 offset:6176\n"
      "s_mov_b32 m0, s68\n"
      "s_add_u32 s68, s68, 4096\n"
      "global_load_lds_dwordx4 v217, s[66:67]\n"
      "s_add_u32 s66, s66, 131072\n"
      "s_addc_u32 s67, s67, 0\n"
      "ds_read_b64_tr_b16 a[200:201], v223 offset:16416\n"
      "ds_read_b64_tr_b16 a[202:203], v223 offset:20512\n"
      "s_mov_b32 m0, s68\n"
      "s_add_u32 s68, s68, 4096\n"
      "global_load_lds_dwordx4 v217, s[66:67]\n"
      "ds_read_b64_tr_b16 a[204:205], v223 offset:18464\n"
      "ds_read_b64_tr_b16 a[206:207], v223 offset:22560\n"
      "s_waitcnt lgkmcnt(0)\n"
      "s_waitcnt lgkmcnt(0)\n"
      "s_waitcnt vmcnt(16)\n"
      "s_barrier\n"
      "v_add_u32 v223, 32768, v220\n"
      "s_mov_b64 s[66:67], %[pwv]\n"
      "s_lshl_b32 s68, s40, 10\n"
      "s_add_u32 s68, s68, 0\n"
      "s_mov_b32 m0, s68\n"
      "s_add_u32 s68, s68, 4096\n"
      "global_load_lds_dwordx4 v218, s[66:67]\n"
      "s_add_u32 s66, s66, 262144\n"
      "s_addc_u32 s67, s67, 0\n"
      "ds_read_b64_tr_b16 a[144:145], v223 offset:0\n"
      "ds_read_b64_tr_b16 a[146:147], v223 offset:4096\n"
      "s_mov_b32 m0, s68\n"
      "s_add_u32 s68, s68, 4096\n"
      "global_load_lds_dwordx4 v218, s[66:67]\n"
      "s_add_u32 s66, s66, 262144\n"
      "s_addc_u32 s67, s67, 0\n"
      "ds_read_b64_tr_b16 a[148:149], v223 offset:2048\n"
      "ds_read_b64_tr_b16 a[150:151], v223 offset:6144\n"
      "s_mov_b32 m0, s68\n"
      "s_add_u32 s68, s68, 4096\n"
      "global_load_lds_dwordx4 v218, s[66:67]\n"
      "s_add_u32 s66, s66, 262144\n"
      "s_addc_u32 s67, s67, 0\n"
      "ds_read_b64_tr_b16 a[152:153], v223 offset:16384\n"
      "ds_read_b64_tr_b16 a[154:155], v223 offset:20480\n"
      "s_mov_b32 m0, s68\n"
      "s_add_u32 s68, s68, 4096\n"
      "global_load_lds_dwordx4 v218, s[66:67]\n"
      "s_add_u32 s66, s66, 262144\n"
      "s_addc_u32 s67, s67, 0\n"
      "ds_read_b64_tr_b16 a[156:157], v223 offset:18432\n"
      "ds_read_b64_tr_b16 a[158:159], v223 offset:22528\n"
      "s_waitcnt lgkmcnt(0)\n"
      "s_mov_b32 m0, s68\n"
      "s_add_u32 s68, s68, 4096\n"
      "global_load_lds_dwordx4 v218, s[66:67]\n"
      "s_add_u32 s66, s66, 262144\n"
      "s_addc_u32 s67, s67, 0\n"
      "ds_read_b64_tr_b16 a[208:209], v223 offset:32\n"
      "ds_read_b64_tr_b16 a[210:211], v223 offset:4128\n"
      "s_mov_b32 m0, s68\n"
      "s_add_u32 s68, s68, 4096\n"
      "global_load_lds_dwordx4 v218, s[66:67]\n"
      "s_add_u32 s66, s66, 262144\n"
      "s_addc_u32 s67, s67, 0\n"
      "ds_read_b64_tr_b16 a[212:213], v223 offset:2080\n"
      "ds_read_b64_tr_b16 a[214:215], v223 offset:6176\n"
      "s_mov_b32 m0, s68\n"
      "s_add_u32 s68, s68, 4096\n"
      "global_load_lds_dwordx4 v218, s[66:67]\n"
      "s_add_u32 s66, s66, 262144\n"
      "s_addc_u32 s67, s67, 0\n"
      "ds_read_b64_tr_b16 a[216:217], v223 offset:16416\n"
      "ds_read_b64_tr_b16 a[218:219], v223 offset:20512\n"
      "s_mov_b32 m0, s68\n"
      "s_add_u32 s68, s68, 4096\n"
      "global_load_lds_dwordx4 v218, s[66:67]\n"
      "ds_read_b64_tr_b16 a[220:221], v223 offset:18464\n"
      "ds_read_b64_tr_b16 a[222:223], v223 offset:22560\n"
      "s_waitcnt lgkmcnt(0)\n"
      "s_waitcnt lgkmcnt(0)\n"
      "s_waitcnt vmcnt(16)\n"
      "s_barrier\n"
      "v_add_u32 v223, 65536, v220\n"
      "s_mov_b64 s[66:67], %[pwv]\n"
      "s_add_u32 s66, s66, 2097152\n"
      "s_addc_u32 s67, s67, 0\n"
      "s_lshl_b32 s68, s40, 10\n"
      "s_add_u32 s68, s68, 32768\n"
      "s_mov_b32 m0, s68\n"
      "s_add_u32 s68, s68, 4096\n"
      "global_load_lds_dwordx4 v218, s[66:67]\n"
      "s_add_u32 s66, s66, 262144\n"
      "s_addc_u32 s67, s67, 0\n"
      "ds_read_b64_tr_b16 a[160:161], v223 offset:0\n"
      "ds_read_b64_tr_b16 a[162:163], v223 offset:4096\n"
      "s_mov_b32 m0, s68\n"
      "s_add_u32 s68, s68, 4096\n"
      "global_load_lds_dwordx4 v218, s[66:67]\n"
      "s_add_u32 s66, s66, 262144\n"
      "s_addc_u32 s67, s67, 0\n"
      "ds_read_b64_tr_b16 a[164:165], v223 offset:2048\n"
      "ds_read_b64_tr_b16 a[166:167], v223 offset:6144\n"
      "s_mov_b32 m0, s68\n"
      "s_add_u32 s68, s68, 4096\n"
      "global_load_lds_dwordx4 v218, s[66:67]\n"
      "s_add_u32 s66, s66, 262144\n"
      "s_addc_u32 s67, s67, 0\n"
      "ds_read_b64_tr_b16 a[168:169], v223 offset:16384\n"
      "ds_read_b64_tr_b16 a[170:171], v223 offset:20480\n"
      "s_mov_b32 m0, s68\n"
      "s_add_u32 s68, s68, 4096\n"
      "global_load_lds_dwordx4 v218, s[66:67]\n"
      "s_add_u32 s66, s66, 262144\n"
      "s_addc_u32 s67, s67, 0\n"
      "ds_read_b64_tr_b16 a[172:173], v223 offset:18432\n"
      "ds_read_b64_tr_b16 a[174:175], v223 offset:22528\n"
      "s_waitcnt lgkmcnt(0)\n"
      "s_mov_b32 m0, s68\n"
      "s_add_u32 s68, s68, 4096\n"
      "global_load_lds_dwordx4 v218, s[66:67]\n"
      "s_add_u32 s66, s66, 262144\n"
      "s_addc_u32 s67, s67, 0\n"
      "ds_read_b64_tr_b16 a[224:225], v223 offset:32\n"
      "ds_read_b64_tr_b16 a[226:227], v223 offset:4128\n"
      "s_mov_b32 m0, s68\n"
      "s_add_u32 s68, s68, 4096\n"
      "global_load_lds_dwordx4 v218, s[66:67]\n"
      "s_add_u32 s66, s66, 262144\n"
      "s_addc_u32 s67, s67, 0\n"
      "ds_read_b64_tr_b16 a[228:229], v223 offset:2080\n"
      "ds_read_b64_tr_b16 a[230:231], v223 offset:6176\n"
      "s_mov_b32 m0, s68\n"
      "s_add_u32 s68, s68, 4096\n"
      "global_load_lds_dwordx4 v218, s[66:67]\n"
      "s_add_u32 s66, s66, 262144\n"
      "s_addc_u32 s67, s67, 0\n"
      "ds_read_b64_tr_b16 a[232:233], v223 offset:16416\n"
      "ds_read_b64_tr_b16 a[234:235], v223 offset:20512\n"
      "s_mov_b32 m0, s68\n"
      "s_add_u32 s68, s68, 4096\n"
      "global_load_lds_dwordx4 v218, s[66:67]\n"
      "ds_read_b64_tr_b16 a[236:237], v223 offset:18464\n"
      "ds_read_b64_tr_b16 a[238:239], v223 offset:22560\n"
      "s_waitcnt lgkmcnt(0)\n"
      "s_waitcnt lgkmcnt(0)\n"
      "s_waitcnt vmcnt(16)\n"
      "s_barrier\n"
      "v_add_u32 v223, 98304, v220\n"
      "s_mov_b64 s[66:67], %[pwo]\n"
      "s_lshl_b32 s68, s40, 10\n"
      "s_add_u32 s68, s68, 65536\n"
      "s_mov_b32 m0, s68\n"
      "s_add_u32 s68, s68, 4096\n"
      "global_load_lds_dwordx4 v219, s[66:67]\n"
      "s_add_u32 s66, s66, 4096\n"
      "s_addc_u32 s67, s67, 0\n"
      "ds_read_b64_tr_b16 a[176:177], v223 offset:0\n"
      "ds_read_b64_tr_b16 a[178:179], v223 offset:4096\n"
      "s_mov_b32 m0, s68\n"
      "s_add_u32 s68, s68, 4096\n"
      "global_load_lds_dwordx4 v219, s[66:67]\n"
      "s_add_u32 s66, s66, 4096\n"
      "s_addc_u32 s67, s67, 0\n"
      "ds_read_b64_tr_b16 a[180:181], v223 offset:2048\n"
      "ds_read_b64_tr_b16 a[182:183], v223 offset:6144\n"
      "s_mov_b32 m0, s68\n"
      "s_add_u32 s68, s68, 4096\n"
      "global_load_lds_dwordx4 v219, s[66:67]\n"
      "s_add_u32 s66, s66, 4096\n"
      "s_addc_u32 s67, s67, 0\n"
      "ds_read_b64_tr_b16 a[184:185], v223 offset:16384\n"
      "ds_read_b64_tr_b16 a[186:187], v223 offset:20480\n"
      "s_mov_b32 m0, s68\n"
      "s_add_u32 s68, s68, 4096\n"
      "global_load_lds_dwordx4 v219, s[66:67]\n"
      "s_add_u32 s66, s66, 4096\n"
      "s_addc_u32 s67, s67, 0\n"
      "ds_read_b64_tr_b16 a[188:189], v223 offset:18432\n"
      "ds_read_b64_tr_b16 a[190:191], v223 offset:22528\n"
      "s_waitcnt lgkmcnt(0)\n"
      "s_mov_b32 m0, s68\n"
      "s_add_u32 s68, s68, 4096\n"
      "global_load_lds_dwordx4 v219, s[66:67]\n"
      "s_add_u32 s66, s66, 4096\n"
      "s_addc_u32 s67, s67, 0\n"
      "ds_read_b64_tr_b16 a[240:241], v223 offset:32\n"
      "ds_read_b64_tr_b16 a[242:243], v223 offset:4128\n"
      "s_mov_b32 m0, s68\n"
      "s_add_u32 s68, s68, 4096\n"
      "global_load_lds_dwordx4 v219, s[66:67]\n"
      "s_add_u32 s66, s66, 4096\n"
      "s_addc_u32 s67, s67, 0\n"
      "ds_read_b64_tr_b16 a[244:245], v223 offset:2080\n"
      "ds_read_b64_tr_b16 a[246:247], v223 offset:6176\n"
      "s_mov_b32 m0, s68\n"
      "s_add_u32 s68, s68, 4096\n"
      "global_load_lds_dwordx4 v219, s[66:67]\n"
      "s_add_u32 s66, s66, 4096\n"
      "s_addc_u32 s67, s67, 0\n"
      "ds_read_b64_tr_b16 a[248:249], v223 offset:16416\n"
      "ds_read_b64_tr_b16 a[250:251], v223 offset:20512\n"
      "s_mov_b32 m0, s68\n"
      "s_add_u32 s68, s68, 4096\n"
      "global_load_lds_dwordx4 v219, s[66:67]\n"
      "ds_read_b64_tr_b16 a[252:253], v223 offset:18464\n"
      "ds_read_b64_tr_b16 a[254:255], v223 offset:22560\n"
      "s_waitcnt lgkmcnt(0)\n"
      "s_waitcnt lgkmcnt(0)\n"
      "s_waitcnt vmcnt(16)\n"
      "s_barrier\n"
      "v_add_u32 v223, 0, v221\n"
      "s_mov_b64 s[66:67], %[pwo]\n"
      "s_add_u32 s66, s66, 32768\n"
      "s_addc_u32 s67, s67, 0\n"
      "s_lshl_b32 s68, s40, 10\n"
      "s_add_u32 s68, s68, 98304\n"
      "s_mov_b32 m0, s68\n"
      "s_add_u32 s68, s68, 4096\n"
      "global_load_lds_dwordx4 v219, s[66:67]\n"
      "s_add_u32 s66, s66, 4096\n"
      "s_addc_u32 s67, s67, 0\n"
      "ds_read_b64_tr_b16 v[0:1], v223 offset:0\n"
      "ds_read_b64_tr_b16 v[2:3], v223 offset:2048\n"
      "s_mov_b32 m0, s68\n"
      "s_add_u32 s68, s68, 4096\n"
      "global_load_lds_dwordx4 v219, s[66:67]\n"
      "s_add_u32 s66, s66, 4096\n"
      "s_addc_u32 s67, s67, 0\n"
      "ds_read_b64_tr_b16 v[4:5], v223 offset:1024\n"
      "ds_read_b64_tr_b16 v[6:7], v223 offset:3072\n"
      "s_mov_b32 m0, s68\n"
      "s_add_u32 s68, s68, 4096\n"
      "global_load_lds_dwordx4 v219, s[66:67]\n"
      "s_add_u32 s66, s66, 4096\n"
      "s_addc_u32 s67, s67, 0\n"
      "ds_read_b64_tr_b16 v[8:9], v223 offset:8192\n"
      "ds_read_b64_tr_b16 v[10:11], v223 offset:10240\n"
      "s_mov_b32 m0, s68\n"
      "s_add_u32 s68, s68, 4096\n"
      "global_load_lds_dwordx4 v219, s[66:67]\n"
      "s_add_u32 s66, s66, 4096\n"
      "s_addc_u32 s67, s67, 0\n"
      "ds_read_b64_tr_b16 v[12:13], v223 offset:9216\n"
      "ds_read_b64_tr_b16 v[14:15], v223 offset:11264\n"
      "s_waitcnt lgkmcnt(0)\n"
      "s_mov_b32 m0, s68\n"
      "s_add_u32 s68, s68, 4096\n"
      "global_load_lds_dwordx4 v219, s[66:67]\n"
      "s_add_u32 s66, s66, 4096\n"
      "s_addc_u32 s67, s67, 0\n"
      "ds_read_b64_tr_b16 v[16:17], v223 offset:16384\n"
      "ds_read_b64_tr_b16 v[18:19], v223 offset:18432\n"
      "s_mov_b32 m0, s68\n"
      "s_add_u32 s68, s68, 4096\n"
      "global_load_lds_dwordx4 v219, s[66:67]\n"
      "s_add_u32 s66, s66, 4096\n"
      "s_addc_u32 s67, s67, 0\n"
      "ds_read_b64_tr_b16 v[20:21], v223 offset:17408\n"
      "ds_read_b64_tr_b16 v[22:23], v223 offset:19456\n"
      "s_mov_b32 m0, s68\n"
      "s_add_u32 s68, s68, 4096\n"
      "global_load_lds_dwordx4 v219, s[66:67]\n"
      "s_add_u32 s66, s66, 4096\n"
      "s_addc_u32 s67, s67, 0\n"
      "ds_read_b64_tr_b16 v[24:25], v223 offset:24576\n"
      "ds_read_b64_tr_b16 v[26:27], v223 offset:26624\n"
      "s_mov_b32 m0, s68\n"
      "s_add_u32 s68, s68, 4096\n"
      "global_load_lds_dwordx4 v219, s[66:67]\n"
      "ds_read_b64_tr_b16 v[28:29], v223 offset:25600\n"
      "ds_read_b64_tr_b16 v[30:31], v223 offset:27648\n"
      "s_waitcnt lgkmcnt(0)\n"
      "s_waitcnt lgkmcnt(0)\n"
      "s_waitcnt vmcnt(16)\n"
      "s_barrier\n"
      "v_add_u32 v223, 32768, v221\n"
      "ds_read_b64_tr_b16 v[32:33], v223 offset:0\n"
      "ds_read_b64_tr_b16 v[34:35], v223 offset:2048\n"
      "ds_read_b64_tr_b16 v[36:37], v223 offset:1024\n"
      "ds_read_b64_tr_b16 v[38:39], v223 offset:3072\n"
      "ds_read_b64_tr_b16 v[40:41], v223 offset:8192\n"
      "ds_read_b64_tr_b16 v[42:43], v223 offset:10240\n"
      "ds_read_b64_tr_b16 v[44:45], v223 offset:9216\n"
      "ds_read_b64_tr_b16 v[46:47], v223 offset:11264\n"
      "s_waitcnt lgkmcnt(0)\n"
      "ds_read_b64_tr_b16 v[48:49], v223 offset:16384\n"
      "ds_read_b64_tr_b16 v[50:51], v223 offset:18432\n"
      "ds_read_b64_tr_b16 v[52:53], v223 offset:17408\n"
      "ds_read_b64_tr_b16 v[54:55], v223 offset:19456\n"
      "ds_read_b64_tr_b16 v[56:57], v223 offset:24576\n"
      "ds_read_b64_tr_b16 v[58:59], v223 offset:26624\n"
      "ds_read_b64_tr_b16 v[60:61], v223 offset:25600\n"
      "ds_read_b64_tr_b16 v[62:63], v223 offset:27648\n"
      "s_waitcnt lgkmcnt(0)\n"
      "s_waitcnt lgkmcnt(0)\n"
      "s_waitcnt vmcnt(8)\n"
      "s_barrier\n"
      "v_add_u32 v223, 65536, v222\n"
      "ds_read_b64_tr_b16 v[64:65], v223 offset:0\n"
      "ds_read_b64_tr_b16 v[66:67], v223 offset:2048\n"
      "ds_read_b64_tr_b16 v[72:73], v223 offset:32\n"
      "ds_read_b64_tr_b16 v[74:75], v223 offset:2080\n"
      "ds_read_b64_tr_b16 v[80:81], v223 offset:64\n"
      "ds_read_b64_tr_b16 v[82:83], v223 offset:2112\n"
      "ds_read_b64_tr_b16 v[88:89], v223 offset:96\n"
      "ds_read_b64_tr_b16 v[90:91], v223 offset:2144\n"
      "s_waitcnt lgkmcnt(0)\n"
      "ds_read_b64_tr_b16 v[96:97], v223 offset:128\n"
      "ds_read_b64_tr_b16 v[98:99], v223 offset:2176\n"
      "ds_read_b64_tr_b16 v[104:105], v223 offset:160\n"
      "ds_read_b64_tr_b16 v[106:107], v223 offset:2208\n"
      "ds_read_b64_tr_b16 v[112:113], v223 offset:192\n"
      "ds_read_b64_tr_b16 v[114:115], v223 offset:2240\n"
      "ds_read_b64_tr_b16 v[120:121], v223 offset:224\n"
      "ds_read_b64_tr_b16 v[122:123], v223 offset:2272\n"
      "s_waitcnt lgkmcnt(0)\n"
      "s_waitcnt lgkmcnt(0)\n"
      "s_waitcnt vmcnt(0)\n"
      "s_barrier\n"
      "v_add_u32 v223, 98304, v222\n"
      "ds_read_b64_tr_b16 v[68:69], v223 offset:0\n"
      "ds_read_b64_tr_b16 v[70:71], v223 offset:2048\n"
      "ds_read_b64_tr_b16 v[76:77], v223 offset:32\n"
      "ds_read_b64_tr_b16 v[78:79], v223 offset:2080\n"
      "ds_read_b64_tr_b16 v[84:85], v223 offset:64\n"
      "ds_read_b64_tr_b16 v[86:87], v223 offset:2112\n"
      "ds_read_b64_tr_b16 v[92:93], v223 offset:96\n"
      "ds_read_b64_tr_b16 v[94:95], v223 offset:2144\n"
      "s_waitcnt lgkmcnt(0)\n"
      "ds_read_b64_tr_b16 v[100:101], v223 offset:128\n"
      "ds_read_b64_tr_b16 v[102:103], v223 offset:2176\n"
      "ds_read_b64_tr_b16 v[108:109], v223 offset:160\n"
      "ds_read_b64_tr_b16 v[110:111], v223 offset:2208\n"
      "ds_read_b64_tr_b16 v[116:117], v223 offset:192\n"
      "ds_read_b64_tr_b16 v[118:119], v223 offset:2240\n"
      "ds_read_b64_tr_b16 v[124:125], v223 offset:224\n"
      "ds_read_b64_tr_b16 v[126:127], v223 offset:2272\n"
      "s_waitcnt lgkmcnt(0)\n"
      "s_waitcnt lgkmcnt(0)\n"
      "s_barrier\n"
      "v_readfirstlane_b32 s50, v243\n"
      "s_nop 1\n"
      "s_mul_i32 s50, s50, 0x9e3779b1\n"
      "s_add_u32 s50, s50, 0x7f4a7c15\n"
      "v_lshrrev_b32 v196, 4, v254\n"
      "v_and_b32 v196, 63, v196\n"
      "v_lshlrev_b32 v196, 7, v196\n"
      "v_add_u32 v196, 49024, v196\n"
      "Lbar_%=:\n"
      "buffer_load_dwordx2 v[198:199], v196, %[fr], 0 offen sc1\n"
      "s_add_u32 s42, s42, 1\n"
      "s_cmp_gt_u32 s42, 1500000\n"
      "s_cbranch_scc1 Lbarx_%=\n"
      "s_waitcnt vmcnt(0)\n"
      "v_cmp_ne_u32 vcc, s50, v199\n"
      "s_mov_b64 s[66:67], vcc\n"
      "v_cmp_gt_u32 vcc, 1, v198\n"
      "s_or_b64 vcc, vcc, s[66:67]\n"
      "s_cbranch_vccnz Lbar_%=\n"
      "Lbarx_%=:\n"
      "s_waitcnt vmcnt(0)\n"
      "v_lshrrev_b32 v196, 4, v254\n"
      "v_and_b32 v196, 63, v196\n"
      "v_cmp_gt_u32 vcc, 3, v196\n"
      "s_nop 1\n"
      "v_cndmask_b32 v196, 0, v196, vcc\n"
      "v_lshlrev_b32 v196, 7, v196\n"
      "v_add_u32 v196, 57216, v196\n"
      "Ldec_%=:\n"
      "buffer_load_dwordx2 v[198:199], v196, %[fr], 0 offen sc1\n"
      "s_add_u32 s42, s42, 1\n"
      "s_cmp_gt_u32 s42, 1500000\n"
      "s_cbranch_scc1 Ldecx_%=\n"
      "s_waitcnt vmcnt(0)\n"
      "v_cmp_ne_u32 vcc, s50, v199\n"
      "s_mov_b64 s[66:67], vcc\n"
      "v_cmp_gt_u32 vcc, 0, v198\n"
      "s_or_b64 vcc, vcc, s[66:67]\n"
      "s_cbranch_vccnz Ldec_%=\n"
      "Ldecx_%=:\n"
      "s_waitcnt vmcnt(0)\n"
      "v_readlane_b32 s43, v198, 0\n"
      "v_readlane_b32 s44, v198, 1\n"
      "v_readlane_b32 s69, v198, 2\n"
      "s_nop 1\n"
      "s_lshl_b32 s44, s44, 1\n"
      "s_lshl_b32 s69, s69, 2\n"
      "s_or_b32 s43, s43, s44\n"
      "s_or_b32 s43, s43, s69\n"
      "v_mov_b32 v197, s43\n"
      "s_nop 0\n"
      "v_readfirstlane_b32 s43, v197\n"
      "s_nop 1\n"
      "s_and_b32 s44, s43, 1\n"
      "s_lshl_b32 s44, s44, 13\n"
      "s_and_b32 s69, s43, 2\n"
      "s_lshl_b32 s69, s69, 12\n"
      "s_sub_u32 s58, s69, s44\n"
      "s_lshl_b32 s51, s57, 7\n"
      "s_add_u32 s51, s51, s44\n"
      "s_add_u32 s51, s51, 65536\n"
      "s_lshl_b32 s69, %[xg], 14\n"
      "s_add_u32 s45, s44, s69\n"
      "s_add_u32 s45, s45, 0\n"
      "s_and_b32 s44, s43, 4\n"
      "s_lshl_b32 s44, s44, 11\n"
      "s_add_u32 s48, s44, 4194304\n"
      "s_add_u32 s61, s48, 131072\n"
      "s_lshl_b32 s44, s57, 14\n"
      "s_add_u32 s49, s48, s44\n"
      "s_add_u32 s99, s49, 4096\n"
      "s_lshl_b32 s44, s40, 11\n"
      "s_add_u32 s60, s49, s44\n"
      "s_xor_b32 s60, s60, 0x100000\n"
      "s_lshl_b32 s44, %[h], 11\n"
      "s_add_u32 s46, s44, 60809216\n"
      "s_lshr_b32 s69, s40, 1\n"
      "s_cmp_eq_u32 s69, %[a]\n"
      "s_cselect_b32 s46, s46, 0xe0000000\n"
      "s_lshl_b32 s69, %[a], 10\n"
      "s_add_u32 s47, s44, s69\n"
      "s_lshl_b32 s69, s40, 8\n"
      "s_sub_u32 s47, s47, s69\n"
      "s_add_u32 s47, s47, 65003520\n"
      "s_add_u32 s59, s57, 255\n"
      "s_lshl_b32 s59, s59, 7\n"
      "s_cmp_eq_u32 s40, 0\n"
      "s_cselect_b32 s59, s59, 0xe0000000\n"
      "s_lshl_b32 s44, %[h], 13\n"
      "s_lshl_b32 s69, s40, 10\n"
      "s_add_u32 s44, s44, s69\n"
      "s_add_u32 s52, s44, 10485760\n"
      "s_add_u32 s53, s52, 4096\n"
      "s_lshl_b32 s69, %[h], 13\n"
      "s_add_u32 s73, s69, 10488832\n"
      "s_lshl_b32 s69, s40, 12\n"
      "s_add_u32 s73, s73, s69\n"
      "s_cmp_lt_u32 s40, 2\n"
      "s_cselect_b32 s73, s73, 0xe0000000\n"
      "s_lshl_b32 s98, s40, 10\n"
      "s_add_u32 s98, s98, 22528\n"
      "s_lshl_b32 s69, %[a], 12\n"
      "s_add_u32 s54, s44, s69\n"
      "s_add_u32 s54, s54, 27262976\n"
      "s_mov_b32 s41, 0\n"
      "s_add_u32 s43, s41, 1\n"
      "s_cmp_ge_u32 s55, s43\n"
      "v_and_b32 v228, 0x3ff, v254\n"
      "s_cbranch_scc1 Lkv0ok_%=\n"
      "v_lshrrev_b32 v229, 4, v228\n"
      "v_and_b32 v229, 1, v229\n"
      "v_lshlrev_b32 v229, 12, v229\n"
      "s_add_u32 s44, %[h], 129\n"
      "s_lshl_b32 s44, s44, 7\n"
      "v_add_u32 v229, s44, v229\n"
      "Lkv0_%=:\n"
      "buffer_load_dwordx2 v[226:227], v229, %[fr], 0 offen sc1\n"
      "s_add_u32 s42, s42, 1\n"
      "s_cmp_gt_u32 s42, 1500000\n"
      "s_cbranch_scc1 Lkv0x_%=\n"
      "s_waitcnt vmcnt(0)\n"
      "v_cmp_ne_u32 vcc, s50, v227\n"
      "s_mov_b64 s[66:67], vcc\n"
      "v_cmp_gt_u32 vcc, s43, v226\n"
      "s_or_b64 vcc, vcc, s[66:67]\n"
      "s_cbranch_vccnz Lkv0_%=\n"
      "Lkv0x_%=:\n"
      "s_waitcnt vmcnt(0)\n"
      "v_readlane_b32 s55, v226, 0\n"
      "v_readlane_b32 s44, v226, 1\n"
      "s_nop 1\n"
      "s_min_u32 s55, s55, s44\n"
      "Lkv0ok_%=:\n"
      "buffer_load_dwordx4 v[212:215], v228, %[wr], s52 offen sc1\n"
      "buffer_load_dwordx4 v[216:219], v228, %[wr], s53 offen sc1\n"
      "buffer_load_dwordx4 v[220:223], v228, %[wr], s73 offen sc1\n"
      "buffer_load_dwordx4 v[192:195], v228, %[wr], s54 offen sc1\n"
      "s_add_u32 s52, s52, 262144\n"
      "s_add_u32 s53, s53, 262144\n"
      "s_add_u32 s73, s73, 262144\n"
      "s_add_u32 s54, s54, 262144\n"
      "s_lshl_b32 s43, s40, 11\n"
      "v_add_u32 v224, s43, v255\n"
      "v_add_u32 v225, s98, v255\n"
      "s_waitcnt vmcnt(1)\n"
      "ds_write_b128 v224, v[212:215] offset:16384\n"
      "ds_write_b128 v224, v[216:219] offset:17408\n"
      "ds_write_b128 v225, v[220:223]\n"
      "v_mov_b64 v[160:161], 0\n"
      "v_mov_b64 v[162:163], 0\n"
      "v_mov_b64 v[164:165], 0\n"
      "v_mov_b64 v[166:167], 0\n"
      "v_mov_b64 v[168:169], 0\n"
      "v_mov_b64 v[170:171], 0\n"
      "v_mov_b64 v[172:173], 0\n"
      "v_mov_b64 v[174:175], 0\n"
      "v_mov_b64 v[176:177], 0\n"
      "v_mov_b64 v[178:179], 0\n"
      "v_mov_b64 v[180:181], 0\n"
      "v_mov_b64 v[182:183], 0\n"
      "v_mov_b64 v[184:185], 0\n"
      "v_mov_b64 v[186:187], 0\n"
      "v_mov_b64 v[188:189], 0\n"
      "v_mov_b64 v[190:191], 0\n"
      "v_mov_b64 v[228:229], 0\n"
      "v_mov_b64 v[230:231], 0\n"
      "v_mov_b64 v[204:205], 0\n"
      "v_mov_b64 v[206:207], 0\n"
      "v_mov_b64 v[208:209], 0\n"
      "v_mov_b64 v[210:211], 0\n"
      "v_mov_b64 v[196:197], 0\n"
      "v_mov_b64 v[198:199], 0\n"
      "v_mov_b64 v[200:201], 0\n"
      "v_mov_b64 v[202:203], 0\n"
      "v_lshrrev_b32 v212, 4, v254\n"
      "v_and_b32 v212, 63, v212\n"
      "v_and_b32 v213, 7, v212\n"
      "v_lshlrev_b32 v213, 10, v213\n"
      "v_lshrrev_b32 v212, 3, v212\n"
      "v_lshlrev_b32 v212, 2, v212\n"
      "s_lshl_b32 s43, s40, 8\n"
      "v_add3_u32 v212, v212, v213, s43\n"
      "global_load_dword v128, v212, %[pm0] offset:0\n"
      "global_load_dword v129, v212, %[pm0] offset:32\n"
      "global_load_dword v130, v212, %[pm0] offset:64\n"
      "global_load_dword v131, v212, %[pm0] offset:96\n"
      "global_load_dword v132, v212, %[pm0] offset:128\n"
      "global_load_dword v133, v212, %[pm0] offset:160\n"
      "global_load_dword v134, v212, %[pm0] offset:192\n"
      "global_load_dword v135, v212, %[pm0] offset:224\n"
      "s_waitcnt vmcnt(0)\n"
      "s_branch Lhave_%=\n"
      "Lloop_%=:\n"
      "buffer_load_dwordx4 v[128:131], v254, %[wr], s45 offen sc1\n"
      "buffer_load_dwordx4 v[132:135], v254, %[wr], s45 offen offset:1024 sc1\n"
      "v_mov_b64 v[160:161], 0\n"
      "v_mov_b64 v[162:163], 0\n"
      "v_mov_b64 v[164:165], 0\n"
      "v_mov_b64 v[166:167], 0\n"
      "v_mov_b64 v[168:169], 0\n"
      "v_mov_b64 v[170:171], 0\n"
      "v_mov_b64 v[172:173], 0\n"
      "v_mov_b64 v[174:175], 0\n"
      "v_mov_b64 v[176:177], 0\n"
      "v_mov_b64 v[178:179], 0\n"
      "v_mov_b64 v[180:181], 0\n"
      "v_mov_b64 v[182:183], 0\n"
      "v_mov_b64 v[184:185], 0\n"
      "v_mov_b64 v[186:187], 0\n"
      "v_mov_b64 v[188:189], 0\n"
      "v_mov_b64 v[190:191], 0\n"
      "v_mov_b64 v[228:229], 0\n"
      "v_mov_b64 v[230:231], 0\n"
      "v_mov_b64 v[204:205], 0\n"
      "v_mov_b64 v[206:207], 0\n"
      "v_mov_b64 v[208:209], 0\n"
      "v_mov_b64 v[210:211], 0\n"
      "v_mov_b64 v[196:197], 0\n"
      "v_mov_b64 v[198:199], 0\n"
      "v_mov_b64 v[200:201], 0\n"
      "v_mov_b64 v[202:203], 0\n"
      "s_xor_b32 s48, s48, 0x100000\n"
      "s_xor_b32 s61, s61, 0x100000\n"
      "s_xor_b32 s49, s49, 0x100000\n"
      "s_xor_b32 s99, s99, 0x100000\n"
      "s_add_u32 s51, s51, 65536\n"
      "s_cmp_eq_u32 s41, 30\n"
      "s_cselect_b32 s43, s58, 0\n"
      "s_add_u32 s51, s51, s43\n"
      "s_add_u32 s41, s41, 1\n"
      "s_lshl_b32 s43, s40, 11\n"
      "v_add_u32 v224, s43, v255\n"
      "v_add_u32 v225, s98, v255\n"
      "s_waitcnt vmcnt(3)\n"
      "ds_write_b128 v224, v[212:215] offset:16384\n"
      "ds_write_b128 v224, v[216:219] offset:17408\n"
      "ds_write_b128 v225, v[220:223]\n"
      "s_branch Lgw_%=\n"
      "Lloop3_%=:\n"
      "buffer_load_dwordx4 v[128:131], v254, %[wr], s45 offen sc1\n"
      "buffer_load_dwordx4 v[132:135], v254, %[wr], s45 offen offset:1024 sc1\n"
      "v_mov_b64 v[160:161], 0\n"
      "v_mov_b64 v[162:163], 0\n"
      "v_mov_b64 v[164:165], 0\n"
      "v_mov_b64 v[166:167], 0\n"
      "v_mov_b64 v[168:169], 0\n"
      "v_mov_b64 v[170:171], 0\n"
      "v_mov_b64 v[172:173], 0\n"
      "v_mov_b64 v[174:175], 0\n"
      "v_mov_b64 v[176:177], 0\n"
      "v_mov_b64 v[178:179], 0\n"
      "v_mov_b64 v[180:181], 0\n"
      "v_mov_b64 v[182:183], 0\n"
      "v_mov_b64 v[184:185], 0\n"
      "v_mov_b64 v[186:187], 0\n"
      "v_mov_b64 v[188:189], 0\n"
      "v_mov_b64 v[190:191], 0\n"
      "v_mov_b64 v[228:229], 0\n"
      "v_mov_b64 v[230:231], 0\n"
      "v_mov_b64 v[204:205], 0\n"
      "v_mov_b64 v[206:207], 0\n"
      "v_mov_b64 v[208:209], 0\n"
      "v_mov_b64 v[210:211], 0\n"
      "v_mov_b64 v[196:197], 0\n"
      "v_mov_b64 v[198:199], 0\n"
      "v_mov_b64 v[200:201], 0\n"
      "v_mov_b64 v[202:203], 0\n"
      "s_xor_b32 s48, s48, 0x100000\n"
      "s_xor_b32 s61, s61, 0x100000\n"
      "s_xor_b32 s49, s49, 0x100000\n"
      "s_xor_b32 s99, s99, 0x100000\n"
      "s_add_u32 s51, s51, 65536\n"
      "s_cmp_eq_u32 s41, 30\n"
      "s_cselect_b32 s43, s58, 0\n"
      "s_add_u32 s51, s51, s43\n"
      "s_add_u32 s41, s41, 1\n"
      "s_branch Lgw_%=\n"
      "Lg_%=:\n"
      "buffer_load_dwordx4 v[128:131], v254, %[wr], s45 offen sc1\n"
      "buffer_load_dwordx4 v[132:135], v254, %[wr], s45 offen offset:1024 sc1\n"
      "s_add_u32 s42, s42, 1\n"
      "s_cmp_gt_u32 s42, 1500000\n"
      "s_cbranch_scc1 Lgx_%=\n"
      "Lgw_%=:\n"
      "s_waitcnt vmcnt(0)\n"
      "v_max3_u32 v226, v128, v129, v130\n"
      "v_max3_u32 v227, v131, v132, v133\n"
      "v_max3_u32 v226, v226, v134, v135\n"
      "v_max_u32 v226, v226, v227\n"
      "v_cmp_eq_u32 vcc, -1, v226\n"
      "s_cbranch_vccnz Lg_%=\n"
      "Lhave_%=:\n"
      "ds_write_b128 v254, v[128:131]\n"
      "ds_write_b128 v254, v[132:135] offset:1024\n"
      "s_waitcnt lgkmcnt(0)\n"
      "s_barrier\n"
      "ds_read_b128 v[128:131], v255\n"
      "ds_read_b128 v[132:135], v255 offset:1024\n"
      "ds_read_b128 v[136:139], v255 offset:2048\n"
      "ds_read_b128 v[140:143], v255 offset:3072\n"
      "ds_read_b128 v[144:147], v255 offset:4096\n"
      "ds_read_b128 v[148:151], v255 offset:5120\n"
      "ds_read_b128 v[152:155], v255 offset:6144\n"
      "ds_read_b128 v[156:159], v255 offset:7168\n"
      "v_mov_b64 v[212:213], -1\n"
      "v_mov_b64 v[214:215], -1\n"
      "v_and_b32 v216, 0x3ff, v254\n"
      "v_mov_b32 v218, s41\n"
      "v_mov_b32 v219, s50\n"
      "s_mov_b64 exec, 1\n"
      "buffer_store_dwordx2 v[218:219], v255, %[fr], s59 offen sc1\n"
      "s_mov_b64 exec, -1\n"
      "s_waitcnt lgkmcnt(7)\n"
      "v_smfmac_f32_16x16x64_bf16 v[196:199], v[128:131], a[0:7], v253\n"
      "v_smfmac_f32_16x16x64_bf16 v[200:203], v[128:131], a[64:71], v253\n"
      "s_waitcnt lgkmcnt(6)\n"
      "v_smfmac_f32_16x16x64_bf16 v[196:199], v[132:135], a[8:15], v253\n"
      "v_smfmac_f32_16x16x64_bf16 v[200:203], v[132:135], a[72:79], v253\n"
      "s_waitcnt lgkmcnt(5)\n"
      "v_smfmac_f32_16x16x64_bf16 v[196:199], v[136:139], a[16:23], v253\n"
      "buffer_store_dwordx4 v[212:215], v216, %[wr], s60 offen sc1\n"
      "v_smfmac_f32_16x16x64_bf16 v[200:203], v[136:139], a[80:87], v253\n"
      "s_waitcnt lgkmcnt(4)\n"
      "v_smfmac_f32_16x16x64_bf16 v[196:199], v[140:143], a[24:31], v253\n"
      "v_smfmac_f32_16x16x64_bf16 v[200:203], v[140:143], a[88:95], v253\n"
      "s_waitcnt lgkmcnt(3)\n"
      "v_smfmac_f32_16x16x64_bf16 v[196:199], v[144:147], a[32:39], v253\n"
      "v_smfmac_f32_16x16x64_bf16 v[200:203], v[144:147], a[96:103], v253\n"
      "s_waitcnt lgkmcnt(2)\n"
      "v_smfmac_f32_16x16x64_bf16 v[196:199], v[148:151], a[40:47], v253\n"
      "v_smfmac_f32_16x16x64_bf16 v[200:203], v[148:151], a[104:111], v253\n"
      "s_waitcnt lgkmcnt(1)\n"
      "v_smfmac_f32_16x16x64_bf16 v[196:199], v[152:155], a[48:55], v253\n"
      "buffer_store_dwordx4 v[212:215], v216, %[wr], s60 offen offset:1024 sc1\n"
      "v_smfmac_f32_16x16x64_bf16 v[200:203], v[152:155], a[112:119], v253\n"
      "s_waitcnt lgkmcnt(0)\n"
      "v_smfmac_f32_16x16x64_bf16 v[196:199], v[156:159], a[56:63], v253\n"
      "v_smfmac_f32_16x16x64_bf16 v[200:203], v[156:159], a[120:127], v253\n"
      "v_smfmac_f32_16x16x64_bf16 v[204:207], v[128:131], a[128:135], v253\n"
      "s_add_u32 s45, s45, 65536\n"
      "s_cmp_eq_u32 s41, 31\n"
      "v_smfmac_f32_16x16x64_bf16 v[208:211], v[128:131], a[192:199], v253\n"
      "s_cselect_b32 s43, s58, 0\n"
      "s_add_u32 s45, s45, s43\n"
      "v_smfmac_f32_16x16x64_bf16 v[204:207], v[132:135], a[136:143], v253\n"
      "s_xor_b32 s60, s60, 0x100000\n"
      "v_add_f32 v196, v196, v197\n"
      "v_smfmac_f32_16x16x64_bf16 v[208:211], v[132:135], a[200:207], v253\n"
      "v_add_f32 v198, v198, v199\n"
      "v_cvt_pk_bf16_f32 v232, v196, v198\n"
      "v_smfmac_f32_16x16x64_bf16 v[204:207], v[136:139], a[144:151], v253\n"
      "ds_write_b32 v252, v232\n"
      "v_add_f32 v200, v200, v201\n"
      "v_smfmac_f32_16x16x64_bf16 v[208:211], v[136:139], a[208:215], v253\n"
      "v_add_f32 v202, v202, v203\n"
      "v_cvt_pk_bf16_f32 v233, v200, v202\n"
      "v_smfmac_f32_16x16x64_bf16 v[204:207], v[140:143], a[152:159], v253\n"
      "ds_write_b32 v252, v233 offset:256\n"
      "v_smfmac_f32_16x16x64_bf16 v[208:211], v[140:143], a[216:223], v253\n"
      "v_smfmac_f32_16x16x64_bf16 v[204:207], v[144:147], a[160:167], v253\n"
      "v_smfmac_f32_16x16x64_bf16 v[208:211], v[144:147], a[224:231], v253\n"
      "v_smfmac_f32_16x16x64_bf16 v[204:207], v[148:151], a[168:175], v253\n"
      "v_smfmac_f32_16x16x64_bf16 v[208:211], v[148:151], a[232:239], v253\n"
      "s_waitcnt lgkmcnt(0)\n"
      "s_barrier\n"
      "v_smfmac_f32_16x16x64_bf16 v[204:207], v[152:155], a[176:183], v253\n"
      "ds_read_b64_tr_b16 v[212:213], v250\n"
      "ds_read_b64_tr_b16 v[214:215], v250 offset:64\n"
      "v_smfmac_f32_16x16x64_bf16 v[208:211], v[152:155], a[240:247], v253\n"
      "ds_read_b64_tr_b16 v[216:217], v250 offset:512\n"
      "ds_read_b64_tr_b16 v[218:219], v250 offset:576\n"
      "v_smfmac_f32_16x16x64_bf16 v[204:207], v[156:159], a[184:191], v253\n"
      "ds_read_b64_tr_b16 v[220:221], v250 offset:1024\n"
      "ds_read_b64_tr_b16 v[222:223], v250 offset:1088\n"
      "v_smfmac_f32_16x16x64_bf16 v[208:211], v[156:159], a[248:255], v253\n"
      "ds_read_b64_tr_b16 v[224:225], v250 offset:1536\n"
      "ds_read_b64_tr_b16 v[226:227], v250 offset:1600\n"
      "v_smfmac_f32_16x16x64_bf16 v[228:231], v[128:131], v[0:7], v253\n"
      "ds_read_b128 v[128:131], v255 offset:16384\n"
      "v_smfmac_f32_16x16x64_bf16 v[228:231], v[132:135], v[8:15], v253\n"
      "ds_read_b128 v[132:135], v255 offset:17408\n"
      "v_smfmac_f32_16x16x64_bf16 v[228:231], v[136:139], v[16:23], v253\n"
      "ds_read_b128 v[136:139], v255 offset:18432\n"
      "v_smfmac_f32_16x16x64_bf16 v[228:231], v[140:143], v[24:31], v253\n"
      "ds_read_b128 v[140:143], v255 offset:19456\n"
      "v_add_f32 v204, v204, v205\n"
      "v_smfmac_f32_16x16x64_bf16 v[228:231], v[144:147], v[32:39], v253\n"
      "v_add_f32 v206, v206, v207\n"
      "v_cvt_pk_bf16_f32 v205, v204, v206\n"
      "v_smfmac_f32_16x16x64_bf16 v[228:231], v[148:151], v[40:47], v253\n"
      "ds_write_b32 v251, v205\n"
      "v_add_f32 v208, v208, v209\n"
      "v_smfmac_f32_16x16x64_bf16 v[228:231], v[152:155], v[48:55], v253\n"
      "v_add_f32 v210, v210, v211\n"
      "v_cvt_pk_bf16_f32 v209, v208, v210\n"
      "v_smfmac_f32_16x16x64_bf16 v[228:231], v[156:159], v[56:63], v253\n"
      "ds_write_b32 v251, v209 offset:512\n"
      "ds_read_b128 v[144:147], v255 offset:20480\n"
      "s_waitcnt lgkmcnt(6)\n"
      "v_mfma_f32_16x16x32_bf16 v[232:235], v[128:131], v[212:215], 0\n"
      "ds_read_b128 v[148:151], v255 offset:21504\n"
      "ds_read_b128 v[152:155], v255 offset:22528\n"
      "s_waitcnt lgkmcnt(7)\n"
      "v_mfma_f32_16x16x32_bf16 v[236:239], v[132:135], v[212:215], 0\n"
      "ds_read_b128 v[156:159], v255 offset:23552\n"
      "v_add_f32 v228, v228, v229\n"
      "s_waitcnt lgkmcnt(7)\n"
      "v_mfma_f32_16x16x32_bf16 v[232:235], v[136:139], v[216:219], v[232:235]\n"
      "v_add_f32 v230, v230, v231\n"
      "v_cvt_pk_bf16_f32 v244, v228, v230\n"
      "s_waitcnt lgkmcnt(6)\n"
      "v_mfma_f32_16x16x32_bf16 v[236:239], v[140:143], v[216:219], v[236:239]\n"
      "s_waitcnt lgkmcnt(4)\n"
      "s_barrier\n"
      "s_waitcnt lgkmcnt(3)\n"
      "v_mfma_f32_16x16x32_bf16 v[232:235], v[144:147], v[220:223], v[232:235]\n"
      "ds_read_b64_tr_b16 v[128:129], v249\n"
      "ds_read_b64_tr_b16 v[130:131], v249 offset:128\n"
      "s_waitcnt lgkmcnt(4)\n"
      "v_mfma_f32_16x16x32_bf16 v[236:239], v[148:151], v[220:223], v[236:239]\n"
      "ds_read_b64_tr_b16 v[132:133], v249 offset:1024\n"
      "ds_read_b64_tr_b16 v[134:135], v249 offset:1152\n"
      "s_waitcnt lgkmcnt(5)\n"
      "v_mfma_f32_16x16x32_bf16 v[232:235], v[152:155], v[224:227], v[232:235]\n"
      "ds_read_b64_tr_b16 v[136:137], v249 offset:2048\n"
      "ds_read_b64_tr_b16 v[138:139], v249 offset:2176\n"
      "s_waitcnt lgkmcnt(6)\n"
      "v_mfma_f32_16x16x32_bf16 v[236:239], v[156:159], v[224:227], v[236:239]\n"
      "ds_read_b64_tr_b16 v[140:141], v249 offset:3072\n"
      "ds_read_b64_tr_b16 v[142:143], v249 offset:3200\n"
      "s_waitcnt lgkmcnt(6)\n"
      "v_mfma_f32_16x16x32_bf16 v[240:243], v[128:131], v[212:215], 0\n"
      "buffer_store_dword v205, v252, %[wr], s46 offen sc1\n"
      "s_waitcnt lgkmcnt(4)\n"
      "v_mfma_f32_16x16x32_bf16 v[240:243], v[132:135], v[216:219], v[240:243]\n"
      "buffer_store_dword v209, v252, %[wr], s46 offen offset:256 sc1\n"
      "s_waitcnt lgkmcnt(2)\n"
      "v_mfma_f32_16x16x32_bf16 v[240:243], v[136:139], v[220:223], v[240:243]\n"
      "buffer_store_dword v244, v252, %[wr], s47 offen sc1\n"
      "s_waitcnt lgkmcnt(0)\n"
      "v_mfma_f32_16x16x32_bf16 v[240:243], v[140:143], v[224:227], v[240:243]\n"
      "v_cndmask_b32 v232, v232, v236, s[62:63]\n"
      "v_cndmask_b32 v233, v233, v237, s[62:63]\n"
      "v_cndmask_b32 v234, v234, v238, s[62:63]\n"
      "v_cndmask_b32 v235, v235, v239, s[62:63]\n"
      "v_max3_f32 v213, v232, v233, v234\n"
      "s_cmp_eq_u32 s41, 63\n"
      "s_cbranch_scc1 Ldone_%=\n"
      "s_nop 0\n"
      "v_cndmask_b32 v212, v240, v241, s[62:63]\n"
      "v_max3_f32 v213, v213, v235, v212\n"
      "v_mov_b32 v216, 0x3f803f80\n"
      "v_mov_b32 v217, 0x3f803f80\n"
      "v_max_f32_dpp v213, v213, v213 row_ror:8 row_mask:0xf bank_mask:0xf\n"
      "v_mov_b32 v214, v213\n"
      "v_mov_b32 v218, 0x3f803f80\n"
      "v_mov_b32 v219, 0x3f803f80\n"
      "v_permlane16_swap_b32 v213, v214\n"
      "v_max_f32 v213, v213, v214\n"
      "v_mov_b32 v214, v213\n"
      "v_mov_b32 v225, 0\n"
      "s_nop 0\n"
      "v_permlane32_swap_b32 v213, v214\n"
      "v_max_f32 v213, v213, v214\n"
      "v_mul_f32 v213, s56, v213\n"
      "v_fma_f32 v212, v212, s56, -v213\n"
      "v_fma_f32 v232, v232, s56, -v213\n"
      "v_fma_f32 v233, v233, s56, -v213\n"
      "v_fma_f32 v234, v234, s56, -v213\n"
      "v_fma_f32 v235, v235, s56, -v213\n"
      "v_exp_f32 v212, v212\n"
      "v_exp_f32 v232, v232\n"
      "v_exp_f32 v233, v233\n"
      "v_exp_f32 v234, v234\n"
      "v_exp_f32 v235, v235\n"
      "v_mov_b32_dpp v214, v212 row_ror:8 row_mask:0xf bank_mask:0xf\n"
      "v_cvt_pk_bf16_f32 v220, v232, v233\n"
      "v_cvt_pk_bf16_f32 v221, v234, v235\n"
      "v_cvt_pk_bf16_f32 v224, v212, v214\n"
      "v_mov_b32_dpp v222, v220 row_ror:8 row_mask:0xf bank_mask:0xf\n"
      "v_mov_b32_dpp v223, v221 row_ror:8 row_mask:0xf bank_mask:0xf\n"
      "v_mfma_f32_16x16x16_bf16 v[200:203], v[216:217], v[224:225], 0\n"
      "v_mfma_f32_16x16x16_bf16 v[196:199], v[244:245], v[224:225], 0\n"
      "v_mfma_f32_16x16x32_bf16 v[200:203], v[216:219], v[220:223], v[200:203]\n"
      "v_mfma_f32_16x16x32_bf16 v[196:199], v[192:195], v[220:223], v[196:199]\n"
      "v_bfe_u32 v216, v247, 14, 3\n"
      "v_and_b32 v217, 0x7f, v247\n"
      "v_lshl_add_u32 v226, v216, 2, v217\n"
      "v_mov_b32 v216, 0xf0000000\n"
      "v_cndmask_b32 v226, v226, v216, s[62:63]\n"
      "s_waitcnt vmcnt(3)\n"
      "s_nop 0\n"
      "v_rcp_f32 v200, v200\n"
      "v_mov_b32 v216, 0x3f80\n"
      "v_mul_f32 v196, v196, v200\n"
      "v_mul_f32 v197, v197, v200\n"
      "v_mul_f32 v198, v198, v200\n"
      "v_mul_f32 v199, v199, v200\n"
      "v_cvt_pk_bf16_f32 v214, v196, v197\n"
      "v_cvt_pk_bf16_f32 v215, v198, v199\n"
      "ds_write_b64 v248, v[214:215]\n"
      "v_mov_b32 v217, 0x3f800000\n"
      "v_cndmask_b32 v216, v216, v217, s[64:65]\n"
      "s_waitcnt lgkmcnt(0)\n"
      "s_barrier\n"
      "ds_read_b128 v[196:199], v255 offset:14336\n"
      "v_cndmask_b32 v212, 0, v216, s[74:75]\n"
      "v_cndmask_b32 v213, 0, v216, s[76:77]\n"
      "v_cndmask_b32 v214, 0, v216, s[78:79]\n"
      "v_cndmask_b32 v215, 0, v216, s[80:81]\n"
      "s_waitcnt lgkmcnt(0)\n"
      "v_smfmac_f32_16x16x64_bf16 v[160:163], v[196:199], v[64:71], v253\n"
      "s_nop 0\n"
      "s_nop 0\n"
      "v_smfmac_f32_16x16x64_bf16 v[164:167], v[196:199], v[72:79], v253\n"
      "s_nop 0\n"
      "s_nop 0\n"
      "v_smfmac_f32_16x16x64_bf16 v[168:171], v[196:199], v[80:87], v253\n"
      "s_nop 0\n"
      "s_nop 0\n"
      "v_smfmac_f32_16x16x64_bf16 v[172:175], v[196:199], v[88:95], v253\n"
      "v_add_f32 v160, v160, v161\n"
      "v_add_f32 v162, v162, v163\n"
      "v_smfmac_f32_16x16x64_bf16 v[176:179], v[196:199], v[96:103], v253\n"
      "v_cvt_pk_bf16_f32 v144, v160, v162\n"
      "v_add_f32 v164, v164, v165\n"
      "v_smfmac_f32_16x16x64_bf16 v[180:183], v[196:199], v[104:111], v253\n"
      "v_add_f32 v166, v166, v167\n"
      "v_cvt_pk_bf16_f32 v145, v164, v166\n"
      "v_smfmac_f32_16x16x64_bf16 v[184:187], v[196:199], v[112:119], v253\n"
      "v_add_f32 v168, v168, v169\n"
      "v_add_f32 v170, v170, v171\n"
      "v_smfmac_f32_16x16x64_bf16 v[188:191], v[196:199], v[120:127], v253\n"
      "v_cvt_pk_bf16_f32 v146, v168, v170\n"
      "v_add_f32 v172, v172, v173\n"
      "v_add_f32 v174, v174, v175\n"
      "v_cvt_pk_bf16_f32 v147, v172, v174\n"
      "buffer_store_dwordx4 v[144:147], v247, %[wr], s48 offen sc1\n"
      "v_add_f32 v176, v176, v177\n"
      "v_add_f32 v178, v178, v179\n"
      "v_cvt_pk_bf16_f32 v148, v176, v178\n"
      "v_add_f32 v180, v180, v181\n"
      "v_add_f32 v182, v182, v183\n"
      "v_cvt_pk_bf16_f32 v149, v180, v182\n"
      "v_add_f32 v184, v184, v185\n"
      "v_add_f32 v186, v186, v187\n"
      "v_cvt_pk_bf16_f32 v150, v184, v186\n"
      "v_add_f32 v188, v188, v189\n"
      "v_add_f32 v190, v190, v191\n"
      "v_cvt_pk_bf16_f32 v151, v188, v190\n"
      "buffer_store_dwordx4 v[148:151], v247, %[wr], s61 offen sc1\n"
      "s_add_u32 s46, s46, 65536\n"
      "s_add_u32 s47, s47, 65536\n"
      "s_cmp_eq_u32 s40, 3\n"
      "s_cbranch_scc1 Lred_%=\n"
      "s_add_u32 s43, s41, 2\n"
      "s_cmp_ge_u32 s55, s43\n"
      "v_and_b32 v228, 0x3ff, v254\n"
      "s_cbranch_scc1 Lkv1ok_%=\n"
      "v_lshrrev_b32 v229, 4, v228\n"
      "v_and_b32 v229, 1, v229\n"
      "v_lshlrev_b32 v229, 12, v229\n"
      "s_add_u32 s44, %[h], 129\n"
      "s_lshl_b32 s44, s44, 7\n"
      "v_add_u32 v229, s44, v229\n"
      "Lkv1_%=:\n"
      "buffer_load_dwordx2 v[226:227], v229, %[fr], 0 offen sc1\n"
      "s_add_u32 s42, s42, 1\n"
      "s_cmp_gt_u32 s42, 1500000\n"
      "s_cbranch_scc1 Lkv1x_%=\n"
      "s_waitcnt vmcnt(0)\n"
      "v_cmp_ne_u32 vcc, s50, v227\n"
      "s_mov_b64 s[66:67], vcc\n"
      "v_cmp_gt_u32 vcc, s43, v226\n"
      "s_or_b64 vcc, vcc, s[66:67]\n"
      "s_cbranch_vccnz Lkv1_%=\n"
      "Lkv1x_%=:\n"
      "s_waitcnt vmcnt(0)\n"
      "v_readlane_b32 s55, v226, 0\n"
      "v_readlane_b32 s44, v226, 1\n"
      "s_nop 1\n"
      "s_min_u32 s55, s55, s44\n"
      "Lkv1ok_%=:\n"
      "buffer_load_dwordx4 v[212:215], v228, %[wr], s52 offen sc1\n"
      "buffer_load_dwordx4 v[216:219], v228, %[wr], s53 offen sc1\n"
      "buffer_load_dwordx4 v[220:223], v228, %[wr], s73 offen sc1\n"
      "buffer_load_dwordx4 v[192:195], v228, %[wr], s54 offen sc1\n"
      "s_add_u32 s52, s52, 262144\n"
      "s_add_u32 s53, s53, 262144\n"
      "s_add_u32 s73, s73, 262144\n"
      "s_add_u32 s54, s54, 262144\n"
      "s_barrier\n"
      "s_mov_b32 s44, 13\n"
      "Ldd2_%=:\n"
      "s_sub_u32 s44, s44, 1\n"
      "s_cbranch_scc0 Ldd2_%=\n"
      "s_branch Lloop_%=\n"
      "Lred_%=:\n"
      "s_mov_b32 s44, 26\n"
      "Ldd1_%=:\n"
      "s_sub_u32 s44, s44, 1\n"
      "s_cbranch_scc0 Ldd1_%=\n"
      "Lrl_%=:\n"
      "buffer_load_dwordx4 v[160:163], v255, %[wr], s49 offen sc1\n"
      "buffer_load_dwordx4 v[164:167], v255, %[wr], s49 offen offset:1024 sc1\n"
      "buffer_load_dwordx4 v[168:171], v255, %[wr], s49 offen offset:2048 sc1\n"
      "buffer_load_dwordx4 v[172:175], v255, %[wr], s49 offen offset:3072 sc1\n"
      "buffer_load_dwordx4 v[176:179], v255, %[wr], s99 offen sc1\n"
      "buffer_load_dwordx4 v[180:183], v255, %[wr], s99 offen offset:1024 sc1\n"
      "buffer_load_dwordx4 v[184:187], v255, %[wr], s99 offen offset:2048 sc1\n"
      "buffer_load_dwordx4 v[188:191], v255, %[wr], s99 offen offset:3072 sc1\n"
      "s_add_u32 s42, s42, 1\n"
      "s_cmp_gt_u32 s42, 1500000\n"
      "s_cbranch_scc1 Lrx_%=\n"
      "s_waitcnt vmcnt(7)\n"
      "v_mfma_f32_16x16x32_bf16 v[152:155], v[160:163], v[212:215], 0\n"
      "s_waitcnt vmcnt(6)\n"
      "v_mfma_f32_16x16x32_bf16 v[152:155], v[164:167], v[212:215], v[152:155]\n"
      "s_waitcnt vmcnt(5)\n"
      "v_mfma_f32_16x16x32_bf16 v[152:155], v[168:171], v[212:215], v[152:155]\n"
      "s_waitcnt vmcnt(4)\n"
      "v_mfma_f32_16x16x32_bf16 v[152:155], v[172:175], v[212:215], v[152:155]\n"
      "s_waitcnt vmcnt(3)\n"
      "v_mfma_f32_16x16x32_bf16 v[152:155], v[176:179], v[212:215], v[152:155]\n"
      "s_waitcnt vmcnt(2)\n"
      "v_mfma_f32_16x16x32_bf16 v[152:155], v[180:183], v[212:215], v[152:155]\n"
      "s_waitcnt vmcnt(1)\n"
      "v_mfma_f32_16x16x32_bf16 v[152:155], v[184:187], v[212:215], v[152:155]\n"
      "s_waitcnt vmcnt(0)\n"
      "v_mfma_f32_16x16x32_bf16 v[152:155], v[188:191], v[212:215], v[152:155]\n"
      "s_nop 7\n"
      "v_add_f32 v152, v152, v153\n"
      "v_add_f32 v154, v154, v155\n"
      "v_add_f32 v216, v152, v154\n"
      "v_cvt_pk_bf16_f32 v217, v152, v154\n"
      "v_cmp_u_f32 vcc, v216, v216\n"
      "v_min_u32 v217, -2, v217\n"
      "s_cbranch_vccz Lrx_%=\n"
      "v_max3_u32 v216, v160, v161, v162\n"
      "v_max3_u32 v216, v216, v163, v164\n"
      "v_max3_u32 v216, v216, v165, v166\n"
      "v_max3_u32 v216, v216, v167, v168\n"
      "v_max3_u32 v216, v216, v169, v170\n"
      "v_max3_u32 v216, v216, v171, v172\n"
      "v_max3_u32 v216, v216, v173, v174\n"
      "v_max3_u32 v216, v216, v175, v176\n"
      "v_max3_u32 v216, v216, v177, v178\n"
      "v_max3_u32 v216, v216, v179, v180\n"
      "v_max3_u32 v216, v216, v181, v182\n"
      "v_max3_u32 v216, v216, v183, v184\n"
      "v_max3_u32 v216, v216, v185, v186\n"
      "v_max3_u32 v216, v216, v187, v188\n"
      "v_max3_u32 v216, v216, v189, v190\n"
      "v_max_u32 v216, v216, v191\n"
      "v_cmp_eq_u32 vcc, -1, v216\n"
      "s_cbranch_vccnz Lrl_%=\n"
      "Lrx_%=:\n"
      "buffer_store_dword v217, v226, %[wr], s51 offen sc1\n"
      "s_add_u32 s51, s51, 16384\n"
      "buffer_store_dword v217, v226, %[wr], s51 offen sc1\n"
      "s_add_u32 s51, s51, 16384\n"
      "buffer_store_dword v217, v226, %[wr], s51 offen sc1\n"
      "s_add_u32 s51, s51, 16384\n"
      "buffer_store_dword v217, v226, %[wr], s51 offen sc1\n"
      "s_sub_u32 s51, s51, 49152\n"
      "s_waitcnt vmcnt(4)\n"
      "s_barrier\n"
      "s_add_u32 s43, s41, 2\n"
      "s_cmp_ge_u32 s55, s43\n"
      "v_and_b32 v228, 0x3ff, v254\n"
      "s_cbranch_scc1 Lkv2ok_%=\n"
      "v_lshrrev_b32 v229, 4, v228\n"
      "v_and_b32 v229, 1, v229\n"
      "v_lshlrev_b32 v229, 12, v229\n"
      "s_add_u32 s44, %[h], 129\n"
      "s_lshl_b32 s44, s44, 7\n"
      "v_add_u32 v229, s44, v229\n"
      "Lkv2_%=:\n"
      "buffer_load_dwordx2 v[226:227], v229, %[fr], 0 offen sc1\n"
      "s_add_u32 s42, s42, 1\n"
      "s_cmp_gt_u32 s42, 1500000\n"
      "s_cbranch_scc1 Lkv2x_%=\n"
      "s_waitcnt vmcnt(0)\n"
      "v_cmp_ne_u32 vcc, s50, v227\n"
      "s_mov_b64 s[66:67], vcc\n"
      "v_cmp_gt_u32 vcc, s43, v226\n"
      "s_or_b64 vcc, vcc, s[66:67]\n"
      "s_cbranch_vccnz Lkv2_%=\n"
      "Lkv2x_%=:\n"
      "s_waitcnt vmcnt(0)\n"
      "v_readlane_b32 s55, v226, 0\n"
      "v_readlane_b32 s44, v226, 1\n"
      "s_nop 1\n"
      "s_min_u32 s55, s55, s44\n"
      "Lkv2ok_%=:\n"
      "buffer_load_dwordx4 v[192:195], v228, %[wr], s54 offen sc1\n"
      "s_add_u32 s52, s52, 262144\n"
      "s_add_u32 s53, s53, 262144\n"
      "s_add_u32 s73, s73, 262144\n"
      "s_add_u32 s54, s54, 262144\n"
      "s_mov_b32 s44, 9\n"
      "Ldd3_%=:\n"
      "s_sub_u32 s44, s44, 1\n"
      "s_cbranch_scc0 Ldd3_%=\n"
      "s_branch Lloop3_%=\n"
      "Ldone_%=:\n"
      "s_waitcnt vmcnt(0) lgkmcnt(0)\n"
      "s_barrier\n"
      "v_mov_b32 v212, 64\n"
      "v_mov_b32 v213, s50\n"
      "s_mov_b64 exec, 1\n"
      "buffer_store_dwordx2 v[212:213], v255, %[fr], s59 offen sc1\n"
      "s_mov_b64 exec, -1\n"
      "s_branch Lend_%=\n"
      "Lgx_%=:\n"
      "s_waitcnt vmcnt(0)\n"
      "s_branch Lhave_%=\n"
      "Lend_%=:\n"
      : "+{v0}"(tid)
      : [wr] "s"(wr), [fr] "s"(fr), [pwq] "s"(pwq), [pwk] "s"(pwk), [pwv] "s"(pwv), [pwo] "s"(pwo), [pm0] "s"(P.mem0),
        [h] "s"(h), [a] "s"(a), [xg] "s"(xg)

      : "memory", "vcc", "scc", "s40", "s41", "s42", "s43", "s44", "s45", "s46", "s47", "s48", "s49", "s50", "s51", "s52", "s53", "s54", "s55", "s56", "s57", "s58", "s59", "s60", "s61", "s62", "s63", "s64", "s65", "s66", "s67", "s68", "s69", "s73", "s74", "s75", "s76", "s77", "s78", "s79", "s80", "s81", "s98", "s99", "v1", "v2", "v3", "v4", "v5", "v6", "v7", "v8", "v9", "v10", "v11", "v12", "v13", "v14", "v15", "v16", "v17", "v18", "v19", "v20", "v21", "v22", "v23", "v24", "v25", "v26", "v27", "v28", "v29", "v30", "v31", "v32", "v33", "v34", "v35", "v36", "v37", "v38", "v39", "v40", "v41", "v42", "v43", "v44", "v45", "v46", "v47", "v48", "v49", "v50", "v51", "v52", "v53", "v54", "v55", "v56", "v57", "v58", "v59", "v60", "v61", "v62", "v63", "v64", "v65", "v66", "v67", "v68", "v69", "v70", "v71", "v72", "v73", "v74", "v75", "v76", "v77", "v78", "v79", "v80", "v81", "v82", "v83", "v84", "v85", "v86", "v87", "v88", "v89", "v90", "v91", "v92", "v93", "v94", "v95", "v96", "v97", "v98", "v99", "v100", "v101", "v102", "v103", "v104", "v105", "v106", "v107", "v108", "v109", "v110", "v111", "v112", "v113", "v114", "v115", "v116", "v117", "v118", "v119", "v120", "v121", "v122", "v123", "v124", "v125", "v126", "v127", "v128", "v129", "v130", "v131", "v132", "v133", "v134", "v135", "v136", "v137", "v138", "v139", "v140", "v141", "v142", "v143", "v144", "v145", "v146", "v147", "v148", "v149", "v150", "v151", "v152", "v153", "v154", "v155", "v156", "v157", "v158", "v159", "v160", "v161", "v162", "v163", "v164", "v165", "v166", "v167", "v168", "v169", "v170", "v171", "v172", "v173", "v174", "v175", "v176", "v177", "v178", "v179", "v180", "v181", "v182", "v183", "v184", "v185", "v186", "v187", "v188", "v189", "v190", "v191", "v192", "v193", "v194", "v195", "v196", "v197", "v198", "v199", "v200", "v201", "v202", "v203", "v204", "v205", "v206", "v207", "v208", "v209", "v210", "v211", "v212", "v213", "v214", "v215", "v216", "v217", "v218", "v219", "v220", "v221", "v222", "v223", "v224", "v225", "v226", "v227", "v228", "v229", "v230", "v231", "v232", "v233", "v234", "v235", "v236", "v237", "v238", "v239", "v240", "v241", "v242", "v243", "v244", "v245", "v247", "v248", "v249", "v250", "v251", "v252", "v253", "v254", "v255", "a0", "a1", "a2", "a3", "a4", "a5", "a6", "a7", "a8", "a9", "a10", "a11", "a12", "a13", "a14", "a15", "a16", "a17", "a18", "a19", "a20", "a21", "a22", "a23", "a24", "a25", "a26", "a27", "a28", "a29", "a30", "a31", "a32", "a33", "a34", "a35", "a36", "a37", "a38", "a39", "a40", "a41", "a42", "a43", "a44", "a45", "a46", "a47", "a48", "a49", "a50", "a51", "a52", "a53", "a54", "a55", "a56", "a57", "a58", "a59", "a60", "a61", "a62", "a63", "a64", "a65", "a66", "a67", "a68", "a69", "a70", "a71", "a72", "a73", "a74", "a75", "a76", "a77", "a78", "a79", "a80", "a81", "a82", "a83", "a84", "a85", "a86", "a87", "a88", "a89", "a90", "a91", "a92", "a93", "a94", "a95", "a96", "a97", "a98", "a99", "a100", "a101", "a102", "a103", "a104", "a105", "a106", "a107", "a108", "a109", "a110", "a111", "a112", "a113", "a114", "a115", "a116", "a117", "a118", "a119", "a120", "a121", "a122", "a123", "a124", "a125", "a126", "a127", "a128", "a129", "a130", "a131", "a132", "a133", "a134", "a135", "a136", "a137", "a138", "a139", "a140", "a141", "a142", "a143", "a144", "a145", "a146", "a147", "a148", "a149", "a150", "a151", "a152", "a153", "a154", "a155", "a156", "a157", "a158", "a159", "a160", "a161", "a162", "a163", "a164", "a165", "a166", "a167", "a168", "a169", "a170", "a171", "a172", "a173", "a174", "a175", "a176", "a177", "a178", "a179", "a180", "a181", "a182", "a183", "a184", "a185", "a186", "a187", "a188", "a189", "a190", "a191", "a192", "a193", "a194", "a195", "a196", "a197", "a198", "a199", "a200", "a201", "a202", "a203", "a204", "a205", "a206", "a207", "a208", "a209", "a210", "a211", "a212", "a213", "a214", "a215", "a216", "a217", "a218", "a219", "a220", "a221", "a222", "a223", "a224", "a225", "a226", "a227", "a228", "a229", "a230", "a231", "a232", "a233", "a234", "a235", "a236", "a237", "a238", "a239", "a240", "a241", "a242", "a243", "a244", "a245", "a246", "a247", "a248", "a249", "a250", "a251", "a252", "a253", "a254", "a255");
}

__device__ __forceinline__ void role_probe(const Params& P, rsrc_t wr, rsrc_t fr, unsigned tag, int pg, int wave, int lane) {
  if (wave != 0) return;
  const unsigned page = pg == 0 ? O_MEM + 4 * 65536 : pg == 1 ? O_MEM + 2 * MB + 4 * 65536 : O_PART;
  if (lane < 2 * PROBE_N) st4(wr, page + (lane & 1) * 8192 + (lane >> 1) * 1152 + 128, 0xFFFFFFFFu);
  waitvm0();
  unsigned m0 = 0xffffffffu, m1 = 0xffffffffu;
  for (int i = 0; i < PROBE_N; ++i) {
    m0 = min(m0, timed_load(P.ws + page + i * 1152 + 128));
    m1 = min(m1, timed_load(P.ws + page + 8192 + i * 1152 + 128));
  }
  if (lane == 0) set_flag(fr, FL_DEC + pg, (m1 < m0 ? 1u : 0u) ^ ((PARITY_FLIP >> pg) & 1u), tag);
}

__global__ void __launch_bounds__(256, 1) fused_kernel(Params P) {
  extern __shared__ __attribute__((aligned(16))) char lds_raw[];
  LAS char* lds = (LAS char*)lds_raw;
  const int b = blockIdx.x, xcd = b & 7, slot = b >> 3;
  const int lane = threadIdx.x & 63, wave = __builtin_amdgcn_readfirstlane(threadIdx.x >> 6);
  const rsrc_t wr = __builtin_amdgcn_make_buffer_rsrc(P.ws, 0, O_END, 0x00020000);
  const rsrc_t fr = __builtin_amdgcn_make_buffer_rsrc(P.flags, 0, FLAG_BYTES, 0x00020000);
  bool dead = false;
  { STAMP_DECL; STAMPW(101, 0); }
  if (xcd < 4 && slot < 16) {
    role_chain(P, wr, fr, (slot >> 1) * 4 + xcd, slot & 1, xcd, wave, lane);
    return;
  }
  const unsigned epoch = load_epoch(fr), tag = tag_of(epoch);
  if (xcd < 4) {
    const int q = (slot - 16) * 4 + xcd;
    if (q >= 62) role_probe(P, wr, fr, tag, q - 62, wave, lane);
    if (q == 14) role_probe(P, wr, fr, tag, 2, wave, lane);
    role_ln(P, wr, fr, tag, q, wave, lane);
    if (q < 14) { start_delay(START_DELAY); role_ws<T_M1>(P, wr, fr, tag, lds, q, wave, lane, dead); }
    else if (q < 30) { start_delay(START_DELAY); role_ws<T_M2>(P, wr, fr, tag, lds, q - 14, wave, lane, dead); }
    else if (q < 62) { if (TOUCH && q < 34) touch_pages(P, wr, wave, lane); init_sentinels(wr, fr, tag, 2 * (q - 30) + 1, wave, lane); start_delay(START_DELAY); role_x(P, wr, fr, tag, lds, q - 30, 1, wave, lane, dead); }
  } else {
    const int q = slot * 4 + (xcd - 4);
    const int h = q & 31, t = q >> 5;
    if (t >= 2) role_ln(P, wr, fr, tag, q, wave, lane);
    if (t == 0) role_ws<T_K>(P, wr, fr, tag, lds, h, wave, lane, dead);
    else if (t == 1) role_ws<T_V>(P, wr, fr, tag, lds, h, wave, lane, dead);
    else if (t == 2) { start_delay(START_DELAY / 2); role_ws<T_Q>(P, wr, fr, tag, lds, h, wave, lane, dead); }
    if (t < 3) role_r(P, wr, fr, epoch, tag, lds, h, t, wave, lane, dead);
    else { if (TOUCH && h < 4) touch_pages(P, wr, wave, lane); init_sentinels(wr, fr, tag, 2 * h, wave, lane); start_delay(START_DELAY / 2); role_x(P, wr, fr, tag, lds, h, 0, wave, lane, dead); }
  }
}

extern "C" void kernel_launch(void* const* d_in, const int* in_sizes, int n_in, void* d_out, int out_size, void* d_ws, size_t ws_size, hipStream_t stream) {
  Params P;
  P.x = (const __bf16*)d_in[0]; P.mem0 = (const __bf16*)d_in[1]; P.Wq = (const __bf16*)d_in[2]; P.Wk = (const __bf16*)d_in[3];
  P.Wv = (const __bf16*)d_in[4]; P.Wo = (const __bf16*)d_in[5];
  P.w1 = (const __bf16*)d_in[8]; P.b1 = (const __bf16*)d_in[9]; P.w2 = (const __bf16*)d_in[10]; P.b2 = (const __bf16*)d_in[11];
  P.out = (__bf16*)d_out; P.flags = (char*)d_ws;
  P.ws = (char*)(((size_t)d_ws + FLAG_BYTES + (2u << 20) - 1) & ~(size_t)((2u << 20) - 1));
  (void)hipFuncSetAttribute((const void*)fused_kernel, hipFuncAttributeMaxDynamicSharedMemorySize, LDS_BYTES);
  hipLaunchKernelGGL(fused_kernel, dim3(256), dim3(256), LDS_BYTES, stream, P);
}
```
